```python
import torch

def _softmax(x, axis=-1):
    return torch.softmax(x, dim=axis)
def _gelu(x):
    return torch.nn.functional.gelu(x, approximate="tanh")
def _silu(x):
    return torch.nn.functional.silu(x)
def _sigmoid(x):
    return torch.sigmoid(x)
def _layernorm(x, axis=-1, eps=1e-5):
    m = x.mean(dim=axis, keepdim=True)
    v = x.var(dim=axis, keepdim=True, unbiased=False)
    return (x - m) / torch.sqrt(v + eps)
def _scatter_add(tgt, idx, src, axis=0):
    return tgt.clone().index_add_(axis, idx, src)
def _segment_sum(src, seg, n):
    out = torch.zeros((n,) + src.shape[1:], dtype=src.dtype)
    return out.index_add_(0, seg, src)
def _pad(x, pw):
    flat = tuple(v for pair in reversed(pw) for v in pair)
    return torch.nn.functional.pad(x, flat)

kernel_name = 'gated_mix__sliding_local_dilated__sliding_block__prompt'

def setup_inputs():

    _w = (lambda a: (lambda t: t.to(torch.bfloat16) if t.is_floating_point() else t)(torch.as_tensor(a)))
    def _A_setup():
        B, S, D = 1, 2048, 512
        return {
            "x": _w((lambda *s: torch.randn(*s, dtype=torch.bfloat16) * 0.05)(B, S, D)),
            "wqkv": _w((lambda *s: torch.randn(*s, dtype=torch.bfloat16) * 1.6)(D, 3 * D)),
            "wo": _w((lambda *s: torch.randn(*s, dtype=torch.bfloat16) * 0.05)(D, D)),
        }
    _A_in = _A_setup()

    def _B_setup():
        B, S, D = 1, 2048, 512
        return {
            "x": _w((lambda *s: torch.randn(*s, dtype=torch.bfloat16) * 0.05)(B, S, D)),
            "wqkv": _w((lambda *s: torch.randn(*s, dtype=torch.bfloat16) * 1.6)(D, 3 * D)),
            "wo": _w((lambda *s: torch.randn(*s, dtype=torch.bfloat16) * 0.05)(D, D)),
        }
    _B_in = _B_setup()

    return {
        "A_x": _A_in["x"],
        "A_wqkv": _A_in["wqkv"],
        "A_wo": _A_in["wo"],
        "B_x": _B_in["x"],
        "B_wqkv": _B_in["wqkv"],
        "B_wo": _B_in["wo"],
        "gate_w": _w((lambda *s: torch.randn(*s, dtype=torch.bfloat16) * 0.05)(512) * 16.0),
    }

def reference(A_x, A_wqkv, A_wo, B_x, B_wqkv, B_wo, gate_w):
    def _A_ref(x, wqkv, wo):
        B, S, D = x.shape
        H = 32; Dh = D // H
        W, dil = 8, 2
        qkv = (lambda x, *d: x.permute(*d))((x @ wqkv).reshape(B, S, 3, H, Dh), 2, 0, 3, 1, 4)
        q, k, v = qkv[0], qkv[1], qkv[2]
        scale = Dh ** -0.5
        pos = torch.arange(S)
        off = torch.arange(W) * dil
        idx = pos[:, None] - off[None, :]
        mask = idx >= 0
        idx_c = torch.where(mask, idx, 0).reshape(-1)
        kg = k[:, :, idx_c, :].reshape(B, H, S, W, Dh)
        vg = v[:, :, idx_c, :].reshape(B, H, S, W, Dh)
        logits = torch.einsum("bhsd,bhswd->bhsw", q, kg) * scale
        logits = torch.where(mask[None, None, :, :], logits, -1e9)
        attn = _softmax(logits, axis=-1)
        o = torch.einsum("bhsw,bhswd->bhsd", attn, vg)
        return (lambda x, *d: x.permute(*d))(o, 0, 2, 1, 3).reshape(B, S, D) @ wo

    def _B_ref(x, wqkv, wo):
        B, S, D = x.shape
        H = 32; Dh = D // H; W = 256
        qkv = (lambda x, *d: x.permute(*d))((x @ wqkv).reshape(B, S, 3, H, Dh), 2, 0, 3, 1, 4)
        q, k, v = qkv[0], qkv[1], qkv[2]
        scores = torch.einsum("bhqd,bhkd->bhqk", q, k) * (Dh ** -0.5)
        idx = torch.arange(S)
        mask = (idx[:, None] - idx[None, :] >= 0) & (idx[:, None] - idx[None, :] < W)
        scores = torch.where(mask[None, None], scores, -1e9)
        p = _softmax(scores, axis=-1)
        o = torch.einsum("bhqk,bhkd->bhqd", p, v)
        return (lambda x, *d: x.permute(*d))(o, 0, 2, 1, 3).reshape(B, S, D) @ wo

    x = A_x
    g = _sigmoid((x * gate_w).sum(-1, keepdims=True))
    a = _A_ref(x, A_wqkv, A_wo)
    b = _B_ref(x, B_wqkv, B_wo)
    return g * a + (1 - g) * b
```

```cpp
#include <hip/hip_runtime.h>
#include <hip/hip_bf16.h>
typedef unsigned short u16;
__device__ __forceinline__ float bf2f(u16 v){ return __uint_as_float(((unsigned)v)<<16); }
__device__ __forceinline__ u16 f2bf(float x){ unsigned u=__float_as_uint(x); u += 0x7fffu + ((u>>16)&1u); return (u16)(u>>16); }
constexpr int S=2048, D=512, H=32, DH=16;
__global__ void k_qkv(const u16* x, const u16* wa, const u16* wb, float* qkv){
  int n = blockIdx.x*256+threadIdx.x; int s0 = blockIdx.y*8;
  const u16* w = n<1536? wa+n : wb+(n-1536);
  float acc[8]={0,0,0,0,0,0,0,0};
  for(int k=0;k<D;k++){ float wv=bf2f(w[(size_t)k*1536]);
    for(int i=0;i<8;i++) acc[i]+=bf2f(x[(s0+i)*D+k])*wv; }
  for(int i=0;i<8;i++) qkv[(size_t)(s0+i)*3072+n]=bf2f(f2bf(acc[i]));
}
__global__ void k_attn(const float* qkv, const u16* x, const u16* gw, float* o){
  int t = blockIdx.x*256+threadIdx.x;
  int s=t>>6, br=(t>>5)&1, h=t&31;
  const float* base=qkv+br*1536+h*16;
  float q[16]; for(int d=0;d<16;d++) q[d]=base[(size_t)s*3072+d];
  int n = br? 256:8, st = br?1:2;
  float m=-1e30f;
  for(int j=0;j<n;j++){ int p=s-j*st; if(p<0)break; float a=0; for(int d=0;d<16;d++) a+=q[d]*base[(size_t)p*3072+512+d]; m=fmaxf(m,a*0.25f);}
  float l=0, acc[16]; for(int d=0;d<16;d++) acc[d]=0;
  for(int j=0;j<n;j++){ int p=s-j*st; if(p<0)break; float a=0; for(int d=0;d<16;d++) a+=q[d]*base[(size_t)p*3072+512+d]; float e=__expf(a*0.25f-m); l+=e; for(int d=0;d<16;d++) acc[d]+=e*base[(size_t)p*3072+1024+d];}
  float g=0; for(int k=0;k<D;k++) g+=bf2f(x[s*D+k])*bf2f(gw[k]);
  g=1.f/(1.f+__expf(-g)); if(br) g=1.f-g;
  for(int d=0;d<16;d++) o[(size_t)s*1024+br*512+h*16+d]=acc[d]/l*g;
}
__global__ void k_out(const float* o, const u16* woa, const u16* wob, u16* out){
  int n = blockIdx.x*256+threadIdx.x; int s0=blockIdx.y*4;
  float acc[4]={0,0,0,0};
  for(int k=0;k<1024;k++){ float wv = bf2f(k<512? woa[k*512+n] : wob[(k-512)*512+n]);
    for(int i=0;i<4;i++) acc[i]+=o[(size_t)(s0+i)*1024+k]*wv; }
  for(int i=0;i<4;i++) out[(s0+i)*512+n]=f2bf(acc[i]);
}
extern "C" void kernel_launch(void* const* d_in, const int* in_sizes, int n_in, void* d_out, int out_size, void* d_ws, size_t ws_size, hipStream_t stream){
  const u16* x=(const u16*)d_in[0]; const u16* wa=(const u16*)d_in[1]; const u16* woa=(const u16*)d_in[2];
  const u16* wb=(const u16*)d_in[4]; const u16* wob=(const u16*)d_in[5]; const u16* gw=(const u16*)d_in[6];
  float* qkv=(float*)d_ws; float* o=qkv+(size_t)S*3072;
  k_qkv<<<dim3(12,S/8),256,0,stream>>>(x,wa,wb,qkv);
  k_attn<<<S*64/256,256,0,stream>>>(qkv,x,gw,o);
  k_out<<<dim3(2,S/4),256,0,stream>>>(o,woa,wob,(u16*)d_out);
}
```

```cpp
#include <hip/hip_runtime.h>

__global__ __launch_bounds__(256, 1) void gated_mix_fused(const void* x, const void* a_wqkv, const void* a_wo, const void* b_wqkv,
                                                          const void* b_wo, const void* gate_w, void* out, void* ws) {
  __shared__ __attribute__((aligned(1024))) char lds[163840];
  const unsigned wave = __builtin_amdgcn_readfirstlane(threadIdx.x >> 6);
  asm volatile(
    "s_mov_b64 s[8:9], %0\n"
    "s_load_dwordx16 s[12:27], s[8:9], 0x0\n"
    "s_mov_b32 s10, %1\n"
    "s_mov_b32 s11, %2\n"
    "v_mbcnt_lo_u32_b32 v208, -1, 0\n"
    "s_and_b32 s35, s10, 7\n"
    "s_lshr_b32 s33, s10, 3\n"
    "v_mbcnt_hi_u32_b32 v208, -1, v208\n"
    "s_and_b32 s28, s35, 1\n"
    "s_lshl_b32 s28, s28, 4\n"
    "s_and_b32 s63, s33, 15\n"
    "s_or_b32 s28, s28, s63\n"
    "s_and_b32 s29, s35, 6\n"
    "s_lshr_b32 s63, s33, 4\n"
    "s_or_b32 s29, s29, s63\n"
    "s_lshl_b32 s30, s35, 2\n"
    "s_lshr_b32 s63, s33, 3\n"
    "s_or_b32 s30, s30, s63\n"
    "s_and_b32 s31, s33, 7\n"
    "s_add_u32 s33, s28, s29\n"
    "s_and_b32 s33, s33, 7\n"
    "s_lshl_b32 s33, s33, 7\n"
    "s_mul_i32 s34, s33, 1536\n"
    "s_lshl_b32 s57, s29, 2\n"
    "s_or_b32 s57, s57, s11\n"
    "s_and_b32 s35, s11, 1\n"
    "s_lshl_b32 s35, s35, 2\n"
    "v_lshrrev_b32 v0, 3, v208\n"
    "v_lshrrev_b32 v1, 3, v208\n"
    "v_and_b32 v1, 2, v1\n"
    "v_and_b32 v2, 7, v208\n"
    "v_or_b32 v1, s35, v1\n"
    "v_xor_b32 v1, v1, v2\n"
    "v_mul_u32_u24 v3, 3072, v0\n"
    "v_lshl_add_u32 v224, v1, 4, v3\n"
    "v_add_u32 v225, 23552, v224\n"
    "v_add_u32 v226, 47104, v224\n"
    "v_add_u32 v227, 70656, v224\n"
    "s_lshl_b32 s35, s11, 14\n"
    "v_lshrrev_b32 v1, 4, v208\n"
    "v_xor_b32 v1, v1, v2\n"
    "v_lshl_add_u32 v3, v0, 10, s35\n"
    "v_lshl_add_u32 v228, v1, 4, v3\n"
    "v_xor_b32 v229, 64, v228\n"
    "v_add_u32 v229, 7168, v229\n"
    "v_lshl_add_u32 v4, v208, 4, s33\n"
    "v_and_b32 v4, 0x3ff, v4\n"
    "s_lshr_b32 s35, s11, 1\n"
    "s_mul_i32 s60, s35, 98304\n"
    "s_add_u32 s58, s60, 1024\n"
    "s_add_u32 s59, s60, 2048\n"
    "s_lshl_b32 s61, s11, 12\n"
    "s_lshl_b32 s62, s11, 11\n"
    "s_add_u32 s62, s62, 65536\n"
    "s_mov_b32 s39, 0x20000\n"
    "s_mov_b32 s43, 0x20000\n"
    "s_mov_b32 s42, 65536\n"
    "s_lshl_b32 s35, s29, 7\n"
    "s_sub_u32 s38, 1572864, s35\n"
    "s_lshl_b32 s63, s28, 16\n"
    "s_waitcnt lgkmcnt(0)\n"
    "s_bitcmp1_b32 s11, 0\n"
    "s_cselect_b32 s36, s14, s18\n"
    "s_cselect_b32 s37, s15, s19\n"
    "s_add_u32 s36, s36, s35\n"
    "s_addc_u32 s37, s37, 0\n"
    "s_add_u32 s40, s12, s63\n"
    "s_addc_u32 s41, s13, 0\n"
    "s_add_u32 m0, s62, 0\n"
    "s_add_u32 s35, s34, s58\n"
    "buffer_load_dwordx4 v228, s[40:43], s33 offen lds\n"
    "buffer_load_dwordx4 v229, s[40:43], s33 offen offset:1024 lds\n"
    "s_add_u32 m0, s61, 0\n"
    "s_nop 0\n"
    "buffer_load_dwordx4 v224, s[36:39], s35 offen lds\n"
    "buffer_load_dwordx4 v225, s[36:39], s35 offen offset:1024 lds\n"
    "buffer_load_dwordx4 v226, s[36:39], s35 offen offset:2048 lds\n"
    "buffer_load_dwordx4 v227, s[36:39], s35 offen offset:3072 lds\n"
    "s_mov_b32 m0, 161792\n"
    "s_nop 0\n"
    "global_load_lds_dwordx4 v4, s[22:23]\n"
    "v_and_b32 v0, 31, v208\n"
    "v_lshrrev_b32 v1, 5, v208\n"
    "v_bfe_u32 v2, v208, 1, 3\n"
    "v_xor_b32 v2, v2, v1\n"
    "v_lshlrev_b32 v3, 7, v0\n"
    "s_lshl_b32 s63, s11, 1\n"
    "v_xor_b32 v2, s63, v2\n"
    "v_lshl_add_u32 v3, v2, 4, v3\n"
    "v_add_u32 v230, 65536, v3\n"
    "v_xor_b32 v231, 32, v230\n"
    "v_xor_b32 v232, 64, v230\n"
    "v_xor_b32 v233, 0x60, v230\n"
    "s_lshl_b32 s63, s11, 5\n"
    "s_add_u32 s63, s63, 161792\n"
    "v_lshl_add_u32 v242, v1, 4, s63\n"
    "v_lshlrev_b32 v5, 2, v1\n"
    "v_sub_u32 v240, v0, v5\n"
    "v_bfe_u32 v5, v208, 4, 1\n"
    "v_bfe_u32 v6, v208, 2, 2\n"
    "v_lshrrev_b32 v7, 1, v6\n"
    "v_lshl_or_b32 v7, v5, 1, v7\n"
    "v_xor_b32 v7, s11, v7\n"
    "v_lshlrev_b32 v8, 12, v5\n"
    "v_lshl_add_u32 v8, v1, 10, v8\n"
    "v_lshl_add_u32 v8, v6, 7, v8\n"
    "v_lshl_add_u32 v8, v7, 5, v8\n"
    "v_and_b32 v9, 3, v208\n"
    "v_lshl_add_u32 v8, v9, 3, v8\n"
    "s_and_b32 s63, s11, 1\n"
    "s_lshl_b32 s63, s63, 11\n"
    "s_lshr_b32 s96, s11, 1\n"
    "s_lshl_b32 s96, s96, 13\n"
    "s_or_b32 s63, s63, s96\n"
    "v_add_u32 v234, s63, v8\n"
    "v_xor_b32 v235, 0x800, v234\n"
    "v_xor_b32 v236, 0x2000, v234\n"
    "v_xor_b32 v254, 0x2800, v234\n"
    "v_lshlrev_b32 v237, 4, v208\n"
    "v_add_u32 v238, 0x1000, v237\n"
    "s_add_u32 s33, s33, 128\n"
    "s_and_b32 s33, s33, 0x3ff\n"
    "s_mul_i32 s34, s33, 1536\n"
    "s_add_u32 m0, s62, 8192\n"
    "s_add_u32 s35, s34, s58\n"
    "buffer_load_dwordx4 v228, s[40:43], s33 offen lds\n"
    "buffer_load_dwordx4 v229, s[40:43], s33 offen offset:1024 lds\n"
    "s_add_u32 m0, s61, 16384\n"
    "s_nop 0\n"
    "buffer_load_dwordx4 v224, s[36:39], s35 offen lds\n"
    "buffer_load_dwordx4 v225, s[36:39], s35 offen offset:1024 lds\n"
    "buffer_load_dwordx4 v226, s[36:39], s35 offen offset:2048 lds\n"
    "buffer_load_dwordx4 v227, s[36:39], s35 offen offset:3072 lds\n"
    "s_lshl_b32 s96, s28, 17\n"
    "s_lshl_b32 s97, s57, 10\n"
    "s_add_u32 s96, s96, s97\n"
    "s_add_u32 s96, s96, 1048576\n"
    "s_add_u32 s12, s26, s96\n"
    "s_addc_u32 s13, s27, 0\n"
    "s_mov_b32 s14, 0x18400\n"
    "s_mov_b32 s15, 0x20000\n"
    "v_mov_b32 v212, -1\n"
    "v_mov_b32 v213, -1\n"
    "v_mov_b32 v214, -1\n"
    "v_mov_b32 v215, -1\n"
    "buffer_store_dwordx4 v[212:215], v237, s[12:15], 0 offen sc1\n"
    "s_mov_b32 s96, 32768\n"
    "buffer_store_dwordx4 v[212:215], v237, s[12:15], s96 offen sc1\n"
    "s_mov_b32 s96, 65536\n"
    "buffer_store_dwordx4 v[212:215], v237, s[12:15], s96 offen sc1\n"
    "s_mov_b32 s96, 98304\n"
    "buffer_store_dwordx4 v[212:215], v237, s[12:15], s96 offen sc1\n"
    "s_waitcnt vmcnt(10)\n"
    "s_barrier\n"
    "ds_read_b64_tr_b16 v[112:113], v234 offset:0\n"
    "ds_read_b64_tr_b16 v[114:115], v234 offset:512\n"
    "ds_read_b128 a[0:3], v230 offset:0\n"
    "ds_read_b128 a[4:7], v230 offset:4096\n"
    "ds_read_b64_tr_b16 v[116:117], v235 offset:0\n"
    "ds_read_b64_tr_b16 v[118:119], v235 offset:512\n"
    "ds_read_b128 a[8:11], v231 offset:0\n"
    "ds_read_b128 a[12:15], v231 offset:4096\n"
    "s_waitcnt lgkmcnt(5)\n"
    "v_mfma_f32_32x32x16_bf16 v[0:15], v[112:115], a[0:3], 0\n"
    "ds_read_b64_tr_b16 v[120:121], v236 offset:0\n"
    "ds_read_b64_tr_b16 v[122:123], v236 offset:512\n"
    "ds_read_b128 a[16:19], v232 offset:0\n"
    "ds_read_b128 a[20:23], v232 offset:4096\n"
    "ds_read_b64_tr_b16 v[124:125], v254 offset:0\n"
    "ds_read_b64_tr_b16 v[126:127], v254 offset:512\n"
    "s_waitcnt lgkmcnt(10)\n"
    "v_mfma_f32_32x32x16_bf16 v[16:31], v[112:115], a[4:7], 0\n"
    "ds_read_b128 a[24:27], v233 offset:0\n"
    "ds_read_b128 a[28:31], v233 offset:4096\n"
    "ds_read_b128 v[216:219], v242 offset:0\n"
    "s_add_u32 s33, s33, 128\n"
    "s_and_b32 s33, s33, 0x3ff\n"
    "s_mul_i32 s34, s33, 1536\n"
    "s_add_u32 m0, s62, 16384\n"
    "s_add_u32 s35, s34, s58\n"
    "buffer_load_dwordx4 v228, s[40:43], s33 offen lds\n"
    "buffer_load_dwordx4 v229, s[40:43], s33 offen offset:1024 lds\n"
    "s_add_u32 m0, s61, 32768\n"
    "s_nop 0\n"
    "buffer_load_dwordx4 v224, s[36:39], s35 offen lds\n"
    "s_waitcnt lgkmcnt(10)\n"
    "v_mfma_f32_32x32x16_bf16 v[0:15], v[116:119], a[8:11], v[0:15]\n"
    "buffer_load_dwordx4 v225, s[36:39], s35 offen offset:1024 lds\n"
    "buffer_load_dwordx4 v226, s[36:39], s35 offen offset:2048 lds\n"
    "buffer_load_dwordx4 v227, s[36:39], s35 offen offset:3072 lds\n"
    "s_add_u32 s33, s33, 128\n"
    "s_and_b32 s33, s33, 0x3ff\n"
    "s_mul_i32 s34, s33, 1536\n"
    "s_add_u32 m0, s62, 24576\n"
    "s_add_u32 s35, s34, s58\n"
    "buffer_load_dwordx4 v228, s[40:43], s33 offen lds\n"
    "buffer_load_dwordx4 v229, s[40:43], s33 offen offset:1024 lds\n"
    "s_add_u32 m0, s61, 49152\n"
    "s_nop 0\n"
    "buffer_load_dwordx4 v224, s[36:39], s35 offen lds\n"
    "s_waitcnt lgkmcnt(9)\n"
    "v_mfma_f32_32x32x16_bf16 v[16:31], v[116:119], a[12:15], v[16:31]\n"
    "s_lshl_b32 s96, s29, 5\n"
    "s_add_u32 s96, s96, s28\n"
    "s_lshl_b32 s96, s96, 10\n"
    "v_add_u32 v200, 1, v208\n"
    "v_lshl_add_u32 v200, v200, 10, s96\n"
    "v_mov_b32 v250, s96\n"
    "s_waitcnt vmcnt(13) lgkmcnt(0)\n"
    "s_barrier\n"
    "v_mfma_f32_32x32x16_bf16 v[0:15], v[120:123], a[16:19], v[0:15]\n"
    "ds_read_b64_tr_b16 v[112:113], v234 offset:16384\n"
    "ds_read_b64_tr_b16 v[114:115], v234 offset:16896\n"
    "ds_read_b128 a[32:35], v230 offset:8192\n"
    "v_mfma_f32_32x32x16_bf16 v[16:31], v[120:123], a[20:23], v[16:31]\n"
    "ds_read_b128 a[36:39], v230 offset:12288\n"
    "ds_read_b64_tr_b16 v[116:117], v235 offset:16384\n"
    "ds_read_b64_tr_b16 v[118:119], v235 offset:16896\n"
    "v_mfma_f32_32x32x16_bf16 v[64:79], v[216:219], a[0:3], 0\n"
    "ds_read_b128 a[40:43], v231 offset:8192\n"
    "ds_read_b128 a[44:47], v231 offset:12288\n"
    "buffer_load_dwordx4 v225, s[36:39], s35 offen offset:1024 lds\n"
    "v_mfma_f32_32x32x16_bf16 v[0:15], v[124:127], a[24:27], v[0:15]\n"
    "buffer_load_dwordx4 v226, s[36:39], s35 offen offset:2048 lds\n"
    "buffer_load_dwordx4 v227, s[36:39], s35 offen offset:3072 lds\n"
    "s_lshl_b32 s63, s28, 14\n"
    "v_mfma_f32_32x32x16_bf16 v[16:31], v[124:127], a[28:31], v[16:31]\n"
    "s_add_u32 s63, s63, 524288\n"
    "v_and_b32 v201, 7, v208\n"
    "v_lshl_add_u32 v201, v201, 11, s63\n"
    "v_mfma_f32_32x32x16_bf16 v[80:95], v[216:219], a[4:7], 0\n"
    "s_lshl_b32 s63, s30, 3\n"
    "s_or_b32 s63, s63, s31\n"
    "s_waitcnt lgkmcnt(5)\n"
    "v_mfma_f32_32x32x16_bf16 v[0:15], v[112:115], a[32:35], v[0:15]\n"
    "ds_read_b64_tr_b16 v[120:121], v236 offset:16384\n"
    "ds_read_b64_tr_b16 v[122:123], v236 offset:16896\n"
    "ds_read_b128 a[48:51], v232 offset:8192\n"
    "ds_read_b128 a[52:55], v232 offset:12288\n"
    "ds_read_b64_tr_b16 v[124:125], v254 offset:16384\n"
    "s_waitcnt lgkmcnt(9)\n"
    "v_mfma_f32_32x32x16_bf16 v[16:31], v[112:115], a[36:39], v[16:31]\n"
    "ds_read_b64_tr_b16 v[126:127], v254 offset:16896\n"
    "ds_read_b128 a[56:59], v233 offset:8192\n"
    "ds_read_b128 a[60:63], v233 offset:12288\n"
    "ds_read_b128 v[216:219], v242 offset:128\n"
    "s_waitcnt lgkmcnt(10)\n"
    "v_mfma_f32_32x32x16_bf16 v[0:15], v[116:119], a[40:43], v[0:15]\n"
    "s_add_u32 s33, s33, 128\n"
    "s_and_b32 s33, s33, 0x3ff\n"
    "s_mul_i32 s34, s33, 1536\n"
    "s_add_u32 m0, s62, 0\n"
    "s_add_u32 s35, s34, s58\n"
    "buffer_load_dwordx4 v228, s[40:43], s33 offen lds\n"
    "buffer_load_dwordx4 v229, s[40:43], s33 offen offset:1024 lds\n"
    "s_add_u32 m0, s61, 0\n"
    "s_nop 0\n"
    "buffer_load_dwordx4 v224, s[36:39], s35 offen lds\n"
    "s_lshl_b32 s63, s63, 11\n"
    "s_add_u32 s63, s63, 524288\n"
    "s_waitcnt lgkmcnt(9)\n"
    "v_mfma_f32_32x32x16_bf16 v[16:31], v[116:119], a[44:47], v[16:31]\n"
    "v_mov_b32 v251, s63\n"
    "v_cmp_gt_u32 vcc, 4, v208\n"
    "v_cmp_eq_u32 s[96:97], 4, v208\n"
    "v_cmp_eq_u32 s[98:99], 5, v208\n"
    "s_waitcnt vmcnt(9) lgkmcnt(0)\n"
    "s_barrier\n"
    "v_mfma_f32_32x32x16_bf16 v[0:15], v[120:123], a[48:51], v[0:15]\n"
    "ds_read_b64_tr_b16 v[112:113], v234 offset:32768\n"
    "ds_read_b64_tr_b16 v[114:115], v234 offset:33280\n"
    "ds_read_b128 a[64:67], v230 offset:16384\n"
    "v_mfma_f32_32x32x16_bf16 v[16:31], v[120:123], a[52:55], v[16:31]\n"
    "ds_read_b128 a[68:71], v230 offset:20480\n"
    "ds_read_b64_tr_b16 v[116:117], v235 offset:32768\n"
    "ds_read_b64_tr_b16 v[118:119], v235 offset:33280\n"
    "v_mfma_f32_32x32x16_bf16 v[64:79], v[216:219], a[32:35], v[64:79]\n"
    "ds_read_b128 a[72:75], v231 offset:16384\n"
    "ds_read_b128 a[76:79], v231 offset:20480\n"
    "buffer_load_dwordx4 v225, s[36:39], s35 offen offset:1024 lds\n"
    "v_mfma_f32_32x32x16_bf16 v[0:15], v[124:127], a[56:59], v[0:15]\n"
    "buffer_load_dwordx4 v226, s[36:39], s35 offen offset:2048 lds\n"
    "buffer_load_dwordx4 v227, s[36:39], s35 offen offset:3072 lds\n"
    "v_cndmask_b32 v246, v201, v200, vcc\n"
    "v_mfma_f32_32x32x16_bf16 v[16:31], v[124:127], a[60:63], v[16:31]\n"
    "v_cndmask_b32 v246, v246, v250, s[96:97]\n"
    "v_cndmask_b32 v246, v246, v251, s[98:99]\n"
    "global_load_dwordx2 v[244:245], v246, s[26:27] sc1\n"
    "v_mfma_f32_32x32x16_bf16 v[80:95], v[216:219], a[36:39], v[80:95]\n"
    "s_add_u32 s96, s11, 1\n"
    "s_lshl_b32 s96, s96, 7\n"
    "s_waitcnt lgkmcnt(5)\n"
    "v_mfma_f32_32x32x16_bf16 v[0:15], v[112:115], a[64:67], v[0:15]\n"
    "ds_read_b64_tr_b16 v[120:121], v236 offset:32768\n"
    "ds_read_b64_tr_b16 v[122:123], v236 offset:33280\n"
    "ds_read_b128 a[80:83], v232 offset:16384\n"
    "ds_read_b128 a[84:87], v232 offset:20480\n"
    "ds_read_b64_tr_b16 v[124:125], v254 offset:32768\n"
    "s_waitcnt lgkmcnt(9)\n"
    "v_mfma_f32_32x32x16_bf16 v[16:31], v[112:115], a[68:71], v[16:31]\n"
    "ds_read_b64_tr_b16 v[126:127], v254 offset:33280\n"
    "ds_read_b128 a[88:91], v233 offset:16384\n"
    "ds_read_b128 a[92:95], v233 offset:20480\n"
    "ds_read_b128 v[216:219], v242 offset:256\n"
    "s_waitcnt lgkmcnt(10)\n"
    "v_mfma_f32_32x32x16_bf16 v[0:15], v[116:119], a[72:75], v[0:15]\n"
    "s_add_u32 s33, s33, 128\n"
    "s_and_b32 s33, s33, 0x3ff\n"
    "s_mul_i32 s34, s33, 1536\n"
    "s_add_u32 m0, s62, 8192\n"
    "s_add_u32 s35, s34, s58\n"
    "buffer_load_dwordx4 v228, s[40:43], s33 offen lds\n"
    "buffer_load_dwordx4 v229, s[40:43], s33 offen offset:1024 lds\n"
    "s_add_u32 m0, s61, 16384\n"
    "s_nop 0\n"
    "buffer_load_dwordx4 v224, s[36:39], s35 offen lds\n"
    "v_lshlrev_b32 v202, 3, v208\n"
    "v_sub_u32 v203, 24, v202\n"
    "s_waitcnt lgkmcnt(9)\n"
    "v_mfma_f32_32x32x16_bf16 v[16:31], v[116:119], a[76:79], v[16:31]\n"
    "v_add_u32 v203, s96, v203\n"
    "s_lshl_b32 s63, s29, 4\n"
    "s_add_u32 s63, s63, s11\n"
    "s_lshl_b32 s63, s63, 3\n"
    "s_waitcnt vmcnt(10) lgkmcnt(0)\n"
    "s_barrier\n"
    "v_mfma_f32_32x32x16_bf16 v[0:15], v[120:123], a[80:83], v[0:15]\n"
    "ds_read_b64_tr_b16 v[112:113], v234 offset:49152\n"
    "ds_read_b64_tr_b16 v[114:115], v234 offset:49664\n"
    "ds_read_b128 a[96:99], v230 offset:24576\n"
    "v_mfma_f32_32x32x16_bf16 v[16:31], v[120:123], a[84:87], v[16:31]\n"
    "ds_read_b128 a[100:103], v230 offset:28672\n"
    "ds_read_b64_tr_b16 v[116:117], v235 offset:49152\n"
    "ds_read_b64_tr_b16 v[118:119], v235 offset:49664\n"
    "v_mfma_f32_32x32x16_bf16 v[64:79], v[216:219], a[64:67], v[64:79]\n"
    "ds_read_b128 a[104:107], v231 offset:24576\n"
    "ds_read_b128 a[108:111], v231 offset:28672\n"
    "buffer_load_dwordx4 v225, s[36:39], s35 offen offset:1024 lds\n"
    "v_mfma_f32_32x32x16_bf16 v[0:15], v[124:127], a[88:91], v[0:15]\n"
    "buffer_load_dwordx4 v226, s[36:39], s35 offen offset:2048 lds\n"
    "buffer_load_dwordx4 v227, s[36:39], s35 offen offset:3072 lds\n"
    "s_add_u32 s63, s63, 1024\n"
    "v_mfma_f32_32x32x16_bf16 v[16:31], v[124:127], a[92:95], v[16:31]\n"
    "v_mov_b32 v201, s63\n"
    "v_cndmask_b32 v203, v201, v203, vcc\n"
    "v_add_u32 v247, v246, v203\n"
    "v_mfma_f32_32x32x16_bf16 v[80:95], v[216:219], a[68:71], v[80:95]\n"
    "v_and_b32 v202, 24, v202\n"
    "v_add3_u32 v250, v250, v202, s96\n"
    "s_waitcnt lgkmcnt(5)\n"
    "v_mfma_f32_32x32x16_bf16 v[0:15], v[112:115], a[96:99], v[0:15]\n"
    "ds_read_b64_tr_b16 v[120:121], v236 offset:49152\n"
    "ds_read_b64_tr_b16 v[122:123], v236 offset:49664\n"
    "ds_read_b128 a[112:115], v232 offset:24576\n"
    "ds_read_b128 a[116:119], v232 offset:28672\n"
    "ds_read_b64_tr_b16 v[124:125], v254 offset:49152\n"
    "s_waitcnt lgkmcnt(9)\n"
    "v_mfma_f32_32x32x16_bf16 v[16:31], v[112:115], a[100:103], v[16:31]\n"
    "ds_read_b64_tr_b16 v[126:127], v254 offset:49664\n"
    "ds_read_b128 a[120:123], v233 offset:24576\n"
    "ds_read_b128 a[124:127], v233 offset:28672\n"
    "ds_read_b128 v[216:219], v242 offset:384\n"
    "s_waitcnt lgkmcnt(10)\n"
    "v_mfma_f32_32x32x16_bf16 v[0:15], v[116:119], a[104:107], v[0:15]\n"
    "s_add_u32 s33, s33, 128\n"
    "s_and_b32 s33, s33, 0x3ff\n"
    "s_mul_i32 s34, s33, 1536\n"
    "s_add_u32 m0, s62, 16384\n"
    "s_add_u32 s35, s34, s58\n"
    "buffer_load_dwordx4 v228, s[40:43], s33 offen lds\n"
    "buffer_load_dwordx4 v229, s[40:43], s33 offen offset:1024 lds\n"
    "s_add_u32 m0, s61, 32768\n"
    "s_nop 0\n"
    "buffer_load_dwordx4 v224, s[36:39], s35 offen lds\n"
    "s_lshl_b32 s96, s11, 8\n"
    "s_add_u32 s96, s96, 1024\n"
    "s_waitcnt lgkmcnt(9)\n"
    "v_mfma_f32_32x32x16_bf16 v[16:31], v[116:119], a[108:111], v[16:31]\n"
    "v_bfe_u32 v203, v208, 2, 1\n"
    "v_lshl_add_u32 v203, v203, 7, s96\n"
    "v_add3_u32 v251, v251, v202, v203\n"
    "s_lshl_b32 s96, s30, 17\n"
    "s_lshl_b32 s97, s11, 13\n"
    "s_add_u32 s96, s96, s97\n"
    "s_add_u32 s96, s96, 1048576\n"
    "s_waitcnt vmcnt(10) lgkmcnt(0)\n"
    "s_barrier\n"
    "v_mfma_f32_32x32x16_bf16 v[0:15], v[120:123], a[112:115], v[0:15]\n"
    "ds_read_b64_tr_b16 v[112:113], v234 offset:0\n"
    "ds_read_b64_tr_b16 v[114:115], v234 offset:512\n"
    "ds_read_b128 a[128:131], v230 offset:0\n"
    "v_mfma_f32_32x32x16_bf16 v[16:31], v[120:123], a[116:119], v[16:31]\n"
    "ds_read_b128 a[132:135], v230 offset:4096\n"
    "ds_read_b64_tr_b16 v[116:117], v235 offset:0\n"
    "ds_read_b64_tr_b16 v[118:119], v235 offset:512\n"
    "v_mfma_f32_32x32x16_bf16 v[64:79], v[216:219], a[96:99], v[64:79]\n"
    "ds_read_b128 a[136:139], v231 offset:0\n"
    "ds_read_b128 a[140:143], v231 offset:4096\n"
    "buffer_load_dwordx4 v225, s[36:39], s35 offen offset:1024 lds\n"
    "v_mfma_f32_32x32x16_bf16 v[0:15], v[124:127], a[120:123], v[0:15]\n"
    "buffer_load_dwordx4 v226, s[36:39], s35 offen offset:2048 lds\n"
    "buffer_load_dwordx4 v227, s[36:39], s35 offen offset:3072 lds\n"
    "s_add_u32 s88, s26, s96\n"
    "s_addc_u32 s89, s27, 0\n"
    "s_mov_b32 s90, 0x1a000\n"
    "s_mov_b32 s91, 0x20000\n"
    "v_mfma_f32_32x32x16_bf16 v[16:31], v[124:127], a[124:127], v[16:31]\n"
    "s_lshl_b32 s96, s31, 7\n"
    "s_add_u32 s44, s16, s96\n"
    "s_addc_u32 s45, s17, 0\n"
    "s_add_u32 s48, s20, s96\n"
    "s_addc_u32 s49, s21, 0\n"
    "s_sub_u32 s46, 524288, s96\n"
    "s_mov_b32 s50, s46\n"
    "s_lshl_b32 s98, s30, 16\n"
    "s_lshl_b32 s99, s11, 13\n"
    "s_add_u32 s98, s98, s99\n"
    "s_add_u32 s98, s98, s96\n"
    "v_mfma_f32_32x32x16_bf16 v[80:95], v[216:219], a[100:103], v[80:95]\n"
    "s_add_u32 s86, s24, s98\n"
    "s_addc_u32 s87, s25, 0\n"
    "s_mov_b32 s47, 0x20000\n"
    "s_mov_b32 s51, 0x20000\n"
    "s_mov_b32 s93, 0x1f8\n"
    "s_mov_b32 s92, 0\n"
    "s_add_u32 s96, s30, s31\n"
    "s_lshl_b32 s96, s96, 11\n"
    "s_waitcnt lgkmcnt(5)\n"
    "v_mfma_f32_32x32x16_bf16 v[0:15], v[112:115], a[128:131], v[0:15]\n"
    "ds_read_b64_tr_b16 v[120:121], v236 offset:0\n"
    "ds_read_b64_tr_b16 v[122:123], v236 offset:512\n"
    "ds_read_b128 a[144:147], v232 offset:0\n"
    "ds_read_b128 a[148:151], v232 offset:4096\n"
    "ds_read_b64_tr_b16 v[124:125], v254 offset:0\n"
    "s_waitcnt lgkmcnt(9)\n"
    "v_mfma_f32_32x32x16_bf16 v[16:31], v[112:115], a[132:135], v[16:31]\n"
    "ds_read_b64_tr_b16 v[126:127], v254 offset:512\n"
    "ds_read_b128 a[152:155], v233 offset:0\n"
    "ds_read_b128 a[156:159], v233 offset:4096\n"
    "ds_read_b128 v[216:219], v242 offset:512\n"
    "s_waitcnt lgkmcnt(10)\n"
    "v_mfma_f32_32x32x16_bf16 v[0:15], v[116:119], a[136:139], v[0:15]\n"
    "s_add_u32 s33, s33, 128\n"
    "s_and_b32 s33, s33, 0x3ff\n"
    "s_mul_i32 s34, s33, 1536\n"
    "s_add_u32 m0, s62, 24576\n"
    "s_add_u32 s35, s34, s58\n"
    "buffer_load_dwordx4 v228, s[40:43], s33 offen lds\n"
    "buffer_load_dwordx4 v229, s[40:43], s33 offen offset:1024 lds\n"
    "s_add_u32 m0, s61, 49152\n"
    "s_nop 0\n"
    "buffer_load_dwordx4 v224, s[36:39], s35 offen lds\n"
    "s_add_u32 s18, s96, 0\n"
    "s_and_b32 s18, s18, 0x1fff\n"
    "s_add_u32 s19, s96, 2048\n"
    "s_and_b32 s19, s19, 0x1fff\n"
    "s_waitcnt lgkmcnt(9)\n"
    "v_mfma_f32_32x32x16_bf16 v[16:31], v[116:119], a[140:143], v[16:31]\n"
    "s_add_u32 s22, s96, 4096\n"
    "s_and_b32 s22, s22, 0x1fff\n"
    "s_add_u32 s23, s96, 6144\n"
    "s_and_b32 s23, s23, 0x1fff\n"
    "s_mov_b32 s16, 0x8000\n"
    "s_mov_b32 s17, 0x10000\n"
    "s_mov_b32 s20, 0x18000\n"
    "s_waitcnt vmcnt(9) lgkmcnt(0)\n"
    "s_barrier\n"
    "v_mfma_f32_32x32x16_bf16 v[0:15], v[120:123], a[144:147], v[0:15]\n"
    "ds_read_b64_tr_b16 v[112:113], v234 offset:16384\n"
    "ds_read_b64_tr_b16 v[114:115], v234 offset:16896\n"
    "ds_read_b128 a[160:163], v230 offset:8192\n"
    "v_mfma_f32_32x32x16_bf16 v[16:31], v[120:123], a[148:151], v[16:31]\n"
    "ds_read_b128 a[164:167], v230 offset:12288\n"
    "ds_read_b64_tr_b16 v[116:117], v235 offset:16384\n"
    "ds_read_b64_tr_b16 v[118:119], v235 offset:16896\n"
    "v_mfma_f32_32x32x16_bf16 v[64:79], v[216:219], a[128:131], v[64:79]\n"
    "ds_read_b128 a[168:171], v231 offset:8192\n"
    "ds_read_b128 a[172:175], v231 offset:12288\n"
    "buffer_load_dwordx4 v225, s[36:39], s35 offen offset:1024 lds\n"
    "v_mfma_f32_32x32x16_bf16 v[0:15], v[124:127], a[152:155], v[0:15]\n"
    "buffer_load_dwordx4 v226, s[36:39], s35 offen offset:2048 lds\n"
    "buffer_load_dwordx4 v227, s[36:39], s35 offen offset:3072 lds\n"
    "s_lshl_b32 s24, s11, 17\n"
    "s_lshl_b32 s21, s11, 12\n"
    "s_add_u32 s21, s21, 32768\n"
    "v_mfma_f32_32x32x16_bf16 v[16:31], v[124:127], a[156:159], v[16:31]\n"
    "v_mov_b32 v239, 0x3f803f80\n"
    "v_mov_b32 v243, 0xff800000\n"
    "s_mov_b32 s68, 0xffff\n"
    "s_mov_b32 s69, 0xffff\n"
    "v_mfma_f32_32x32x16_bf16 v[80:95], v[216:219], a[132:135], v[80:95]\n"
    "s_mov_b32 s70, 0xffff0000\n"
    "s_mov_b32 s71, 0xffff0000\n"
    "s_mov_b32 s8, 0x7f4a7c15\n"
    "s_mov_b32 s9, 0x9e3779b9\n"
    "s_waitcnt lgkmcnt(5)\n"
    "v_mfma_f32_32x32x16_bf16 v[0:15], v[112:115], a[160:163], v[0:15]\n"
    "ds_read_b64_tr_b16 v[120:121], v236 offset:16384\n"
    "ds_read_b64_tr_b16 v[122:123], v236 offset:16896\n"
    "ds_read_b128 a[176:179], v232 offset:8192\n"
    "ds_read_b128 a[180:183], v232 offset:12288\n"
    "ds_read_b64_tr_b16 v[124:125], v254 offset:16384\n"
    "s_waitcnt lgkmcnt(9)\n"
    "v_mfma_f32_32x32x16_bf16 v[16:31], v[112:115], a[164:167], v[16:31]\n"
    "ds_read_b64_tr_b16 v[126:127], v254 offset:16896\n"
    "ds_read_b128 a[184:187], v233 offset:8192\n"
    "ds_read_b128 a[188:191], v233 offset:12288\n"
    "ds_read_b128 v[216:219], v242 offset:640\n"
    "s_waitcnt lgkmcnt(10)\n"
    "v_mfma_f32_32x32x16_bf16 v[0:15], v[116:119], a[168:171], v[0:15]\n"
    "s_add_u32 s33, s33, 128\n"
    "s_and_b32 s33, s33, 0x3ff\n"
    "s_mul_i32 s34, s33, 1536\n"
    "s_add_u32 m0, s61, 0\n"
    "s_add_u32 s35, s34, s59\n"
    "buffer_load_dwordx4 v224, s[36:39], s35 offen lds\n"
    "buffer_load_dwordx4 v225, s[36:39], s35 offen offset:1024 lds\n"
    "s_add_u32 s52, s26, 5242880\n"
    "s_addc_u32 s53, s27, 0\n"
    "s_mov_b32 s54, 8388608\n"
    "s_mov_b32 s55, 0x20000\n"
    "s_waitcnt lgkmcnt(9)\n"
    "v_mfma_f32_32x32x16_bf16 v[16:31], v[116:119], a[172:175], v[16:31]\n"
    "s_lshl_b32 s63, s57, 5\n"
    "s_add_u32 s63, s63, s28\n"
    "s_lshl_b32 s63, s63, 13\n"
    "s_add_u32 s84, s52, s63\n"
    "s_addc_u32 s85, s53, 0\n"
    "s_add_u32 s84, s84, 4096\n"
    "s_addc_u32 s85, s85, 0\n"
    "s_sub_u32 s76, s63, 32768\n"
    "s_cmp_ge_u32 s28, 4\n"
    "s_cselect_b32 s76, s76, 0x7ff00000\n"
    "s_cselect_b32 s80, 0x3f803f80, 0\n"
    "s_sub_u32 s77, s63, 24576\n"
    "s_cmp_ge_u32 s28, 3\n"
    "s_cselect_b32 s77, s77, 0x7ff00000\n"
    "s_cselect_b32 s81, 0x3f803f80, 0\n"
    "s_sub_u32 s78, s63, 16384\n"
    "s_cmp_ge_u32 s28, 2\n"
    "s_cselect_b32 s78, s78, 0x7ff00000\n"
    "s_cselect_b32 s82, 0x3f803f80, 0\n"
    "s_waitcnt vmcnt(8) lgkmcnt(0)\n"
    "s_barrier\n"
    "v_mfma_f32_32x32x16_bf16 v[0:15], v[120:123], a[176:179], v[0:15]\n"
    "ds_read_b64_tr_b16 v[112:113], v234 offset:32768\n"
    "ds_read_b64_tr_b16 v[114:115], v234 offset:33280\n"
    "ds_read_b128 a[192:195], v230 offset:16384\n"
    "v_mfma_f32_32x32x16_bf16 v[16:31], v[120:123], a[180:183], v[16:31]\n"
    "ds_read_b128 a[196:199], v230 offset:20480\n"
    "ds_read_b64_tr_b16 v[116:117], v235 offset:32768\n"
    "ds_read_b64_tr_b16 v[118:119], v235 offset:33280\n"
    "v_mfma_f32_32x32x16_bf16 v[64:79], v[216:219], a[160:163], v[64:79]\n"
    "ds_read_b128 a[200:203], v231 offset:16384\n"
    "ds_read_b128 a[204:207], v231 offset:20480\n"
    "v_mfma_f32_32x32x16_bf16 v[0:15], v[124:127], a[184:187], v[0:15]\n"
    "buffer_load_dwordx4 v226, s[36:39], s35 offen offset:2048 lds\n"
    "buffer_load_dwordx4 v227, s[36:39], s35 offen offset:3072 lds\n"
    "s_sub_u32 s79, s63, 8192\n"
    "s_cmp_ge_u32 s28, 1\n"
    "s_cselect_b32 s79, s79, 0x7ff00000\n"
    "s_cselect_b32 s83, 0x3f803f80, 0\n"
    "v_mfma_f32_32x32x16_bf16 v[16:31], v[124:127], a[188:191], v[16:31]\n"
    "s_min_u32 s96, s28, 4\n"
    "s_sub_u32 s97, 4, s96\n"
    "s_bfm_b64 s[74:75], s96, s97\n"
    "v_lshl_add_u64 v[244:245], v[244:245], 0, s[8:9]\n"
    "s_nop 0\n"
    "v_mfma_f32_32x32x16_bf16 v[80:95], v[216:219], a[164:167], v[80:95]\n"
    "v_readlane_b32 s64, v244, 4\n"
    "v_readlane_b32 s65, v245, 4\n"
    "s_waitcnt lgkmcnt(5)\n"
    "v_mfma_f32_32x32x16_bf16 v[0:15], v[112:115], a[192:195], v[0:15]\n"
    "ds_read_b64_tr_b16 v[120:121], v236 offset:32768\n"
    "ds_read_b64_tr_b16 v[122:123], v236 offset:33280\n"
    "ds_read_b128 a[208:211], v232 offset:16384\n"
    "ds_read_b128 a[212:215], v232 offset:20480\n"
    "ds_read_b64_tr_b16 v[124:125], v254 offset:32768\n"
    "s_waitcnt lgkmcnt(9)\n"
    "v_mfma_f32_32x32x16_bf16 v[16:31], v[112:115], a[196:199], v[16:31]\n"
    "ds_read_b64_tr_b16 v[126:127], v254 offset:33280\n"
    "ds_read_b128 a[216:219], v233 offset:16384\n"
    "ds_read_b128 a[220:223], v233 offset:20480\n"
    "ds_read_b128 v[216:219], v242 offset:768\n"
    "s_waitcnt lgkmcnt(10)\n"
    "v_mfma_f32_32x32x16_bf16 v[0:15], v[116:119], a[200:203], v[0:15]\n"
    "s_add_u32 s33, s33, 128\n"
    "s_and_b32 s33, s33, 0x3ff\n"
    "s_mul_i32 s34, s33, 1536\n"
    "s_add_u32 m0, s61, 16384\n"
    "s_add_u32 s35, s34, s59\n"
    "buffer_load_dwordx4 v224, s[36:39], s35 offen lds\n"
    "buffer_load_dwordx4 v225, s[36:39], s35 offen offset:1024 lds\n"
    "v_readlane_b32 s66, v244, 5\n"
    "v_readlane_b32 s67, v245, 5\n"
    "s_waitcnt lgkmcnt(9)\n"
    "v_mfma_f32_32x32x16_bf16 v[16:31], v[116:119], a[204:207], v[16:31]\n"
    "v_and_b32 v200, 31, v208\n"
    "v_lshlrev_b32_e64 v204, v200, -2\n"
    "v_add_u32 v200, 18, v200\n"
    "v_mov_b32 v206, 0x5555\n"
    "s_waitcnt vmcnt(6) lgkmcnt(0)\n"
    "s_barrier\n"
    "v_mfma_f32_32x32x16_bf16 v[0:15], v[120:123], a[208:211], v[0:15]\n"
    "ds_read_b64_tr_b16 v[112:113], v234 offset:49152\n"
    "ds_read_b64_tr_b16 v[114:115], v234 offset:49664\n"
    "ds_read_b128 a[224:227], v230 offset:24576\n"
    "v_mfma_f32_32x32x16_bf16 v[16:31], v[120:123], a[212:215], v[16:31]\n"
    "ds_read_b128 a[228:231], v230 offset:28672\n"
    "ds_read_b64_tr_b16 v[116:117], v235 offset:49152\n"
    "ds_read_b64_tr_b16 v[118:119], v235 offset:49664\n"
    "v_mfma_f32_32x32x16_bf16 v[64:79], v[216:219], a[192:195], v[64:79]\n"
    "ds_read_b128 a[232:235], v231 offset:24576\n"
    "ds_read_b128 a[236:239], v231 offset:28672\n"
    "v_mfma_f32_32x32x16_bf16 v[0:15], v[124:127], a[216:219], v[0:15]\n"
    "buffer_load_dwordx4 v226, s[36:39], s35 offen offset:2048 lds\n"
    "buffer_load_dwordx4 v227, s[36:39], s35 offen offset:3072 lds\n"
    "v_mov_b32 v207, 0\n"
    "v_mfma_f32_32x32x16_bf16 v[16:31], v[124:127], a[220:223], v[16:31]\n"
    "v_lshlrev_b64 v[206:207], v200, v[206:207]\n"
    "v_lshrrev_b32 v200, 3, v208\n"
    "v_and_b32 v200, 4, v200\n"
    "v_mfma_f32_32x32x16_bf16 v[80:95], v[216:219], a[196:199], v[80:95]\n"
    "v_not_b32 v206, v206\n"
    "v_not_b32 v207, v207\n"
    "s_waitcnt lgkmcnt(5)\n"
    "v_mfma_f32_32x32x16_bf16 v[0:15], v[112:115], a[224:227], v[0:15]\n"
    "ds_read_b64_tr_b16 v[120:121], v236 offset:49152\n"
    "ds_read_b64_tr_b16 v[122:123], v236 offset:49664\n"
    "ds_read_b128 a[240:243], v232 offset:24576\n"
    "ds_read_b128 a[244:247], v232 offset:28672\n"
    "ds_read_b64_tr_b16 v[124:125], v254 offset:49152\n"
    "s_waitcnt lgkmcnt(9)\n"
    "v_mfma_f32_32x32x16_bf16 v[16:31], v[112:115], a[228:231], v[16:31]\n"
    "ds_read_b64_tr_b16 v[126:127], v254 offset:49664\n"
    "ds_read_b128 a[248:251], v233 offset:24576\n"
    "ds_read_b128 a[252:255], v233 offset:28672\n"
    "ds_read_b128 v[216:219], v242 offset:896\n"
    "s_waitcnt lgkmcnt(10)\n"
    "v_mfma_f32_32x32x16_bf16 v[0:15], v[116:119], a[232:235], v[0:15]\n"
    "s_add_u32 s33, s33, 128\n"
    "s_and_b32 s33, s33, 0x3ff\n"
    "s_mul_i32 s34, s33, 1536\n"
    "s_add_u32 m0, s61, 32768\n"
    "s_add_u32 s35, s34, s59\n"
    "buffer_load_dwordx4 v224, s[36:39], s35 offen lds\n"
    "buffer_load_dwordx4 v225, s[36:39], s35 offen offset:1024 lds\n"
    "v_lshrrev_b32 v204, v200, v204\n"
    "v_lshrrev_b32 v206, v200, v206\n"
    "s_waitcnt lgkmcnt(9)\n"
    "v_mfma_f32_32x32x16_bf16 v[16:31], v[116:119], a[236:239], v[16:31]\n"
    "v_lshrrev_b32 v207, v200, v207\n"
    "v_bfe_i32 v96, v204, 0, 1\n"
    "v_lshlrev_b32 v96, 23, v96\n"
    "v_bfe_i32 v97, v204, 1, 1\n"
    "s_waitcnt vmcnt(6) lgkmcnt(0)\n"
    "s_barrier\n"
    "v_mfma_f32_32x32x16_bf16 v[0:15], v[120:123], a[240:243], v[0:15]\n"
    "ds_read_b64_tr_b16 v[112:113], v234 offset:0\n"
    "ds_read_b64_tr_b16 v[114:115], v234 offset:512\n"
    "v_mfma_f32_32x32x16_bf16 v[16:31], v[120:123], a[244:247], v[16:31]\n"
    "ds_read_b64_tr_b16 v[116:117], v235 offset:0\n"
    "ds_read_b64_tr_b16 v[118:119], v235 offset:512\n"
    "v_mfma_f32_32x32x16_bf16 v[64:79], v[216:219], a[224:227], v[64:79]\n"
    "buffer_load_dwordx4 v226, s[36:39], s35 offen offset:2048 lds\n"
    "buffer_load_dwordx4 v227, s[36:39], s35 offen offset:3072 lds\n"
    "v_mfma_f32_32x32x16_bf16 v[0:15], v[124:127], a[248:251], v[0:15]\n"
    "v_lshlrev_b32 v97, 23, v97\n"
    "v_bfe_i32 v98, v204, 2, 1\n"
    "v_mfma_f32_32x32x16_bf16 v[16:31], v[124:127], a[252:255], v[16:31]\n"
    "v_lshlrev_b32 v98, 23, v98\n"
    "v_bfe_i32 v99, v204, 3, 1\n"
    "v_mfma_f32_32x32x16_bf16 v[80:95], v[216:219], a[228:231], v[80:95]\n"
    "v_lshlrev_b32 v99, 23, v99\n"
    "v_bfe_i32 v100, v204, 8, 1\n"
    "s_waitcnt lgkmcnt(2)\n"
    "v_mfma_f32_32x32x16_bf16 v[32:47], a[0:3], v[112:115], 0\n"
    "ds_read_b64_tr_b16 v[120:121], v236 offset:0\n"
    "ds_read_b64_tr_b16 v[122:123], v236 offset:512\n"
    "ds_read_b64_tr_b16 v[124:125], v254 offset:0\n"
    "ds_read_b64_tr_b16 v[126:127], v254 offset:512\n"
    "s_add_u32 s33, s33, 128\n"
    "s_and_b32 s33, s33, 0x3ff\n"
    "s_mul_i32 s34, s33, 1536\n"
    "s_add_u32 m0, s61, 49152\n"
    "s_add_u32 s35, s34, s59\n"
    "buffer_load_dwordx4 v224, s[36:39], s35 offen lds\n"
    "buffer_load_dwordx4 v225, s[36:39], s35 offen offset:1024 lds\n"
    "v_lshlrev_b32 v100, 23, v100\n"
    "v_bfe_i32 v101, v204, 9, 1\n"
    "v_mfma_f32_32x32x16_bf16 v[48:63], a[4:7], v[112:115], 0\n"
    "v_lshlrev_b32 v101, 23, v101\n"
    "v_bfe_i32 v102, v204, 10, 1\n"
    "v_lshlrev_b32 v102, 23, v102\n"
    "v_bfe_i32 v103, v204, 11, 1\n"
    "v_cvt_pk_bf16_f32 v152, v0, v1\n"
    "v_cvt_pk_bf16_f32 v153, v2, v3\n"
    "v_cvt_pk_bf16_f32 v154, v4, v5\n"
    "s_waitcnt lgkmcnt(4)\n"
    "v_mfma_f32_32x32x16_bf16 v[32:47], a[8:11], v[116:119], v[32:47]\n"
    "v_cvt_pk_bf16_f32 v155, v6, v7\n"
    "v_cvt_pk_bf16_f32 v156, v16, v17\n"
    "v_cvt_pk_bf16_f32 v157, v18, v19\n"
    "v_cvt_pk_bf16_f32 v158, v20, v21\n"
    "v_cvt_pk_bf16_f32 v159, v22, v23\n"
    "v_lshrrev_b32 v230, 2, v237\n"
    "v_and_b32 v230, 64, v230\n"
    "v_mfma_f32_32x32x16_bf16 v[48:63], a[12:15], v[116:119], v[48:63]\n"
    "v_and_b32 v231, 0x70, v237\n"
    "v_xor_b32 v230, v230, v231\n"
    "v_lshrrev_b32 v231, 7, v237\n"
    "v_lshl_add_u32 v230, v231, 10, v230\n"
    "v_add_u32 v231, 7168, v230\n"
    "v_add_u32 v232, 14336, v230\n"
    "v_add_u32 v233, 21504, v230\n"
    "s_waitcnt vmcnt(6) lgkmcnt(0)\n"
    "s_barrier\n"
    "v_mfma_f32_32x32x16_bf16 v[32:47], a[16:19], v[120:123], v[32:47]\n"
    "ds_read_b64_tr_b16 v[112:113], v234 offset:16384\n"
    "ds_read_b64_tr_b16 v[114:115], v234 offset:16896\n"
    "ds_read_b64_tr_b16 v[116:117], v235 offset:16384\n"
    "ds_read_b64_tr_b16 v[118:119], v235 offset:16896\n"
    "buffer_load_dwordx4 v226, s[36:39], s35 offen offset:2048 lds\n"
    "buffer_load_dwordx4 v227, s[36:39], s35 offen offset:3072 lds\n"
    "v_cvt_pk_bf16_f32 v160, v8, v9\n"
    "v_cvt_pk_bf16_f32 v161, v10, v11\n"
    "v_mfma_f32_32x32x16_bf16 v[48:63], a[20:23], v[120:123], v[48:63]\n"
    "v_cvt_pk_bf16_f32 v162, v12, v13\n"
    "v_cvt_pk_bf16_f32 v163, v14, v15\n"
    "v_cvt_pk_bf16_f32 v164, v24, v25\n"
    "v_cvt_pk_bf16_f32 v165, v26, v27\n"
    "v_cvt_pk_bf16_f32 v166, v28, v29\n"
    "v_cvt_pk_bf16_f32 v167, v30, v31\n"
    "v_lshrrev_b32 v229, 2, v237\n"
    "v_mfma_f32_32x32x16_bf16 v[32:47], a[24:27], v[124:127], v[32:47]\n"
    "v_and_b32 v229, 0x7c, v229\n"
    "v_add_u32 v229, 162816, v229\n"
    "s_lshl_b32 s93, s11, 8\n"
    "v_add_u32 v228, s93, v229\n"
    "s_mov_b32 s93, 0x1f8\n"
    "global_store_dwordx4 v237, v[152:155], s[84:85] offset:-4096 sc1\n"
    "global_store_dwordx4 v237, v[156:159], s[84:85] offset:-3072 sc1\n"
    "s_mov_b64 exec, s[70:71]\n"
    "global_store_dwordx4 v237, v[164:167], s[84:85] offset:-1024 sc1\n"
    "s_mov_b64 exec, -1\n"
    "v_mfma_f32_32x32x16_bf16 v[48:63], a[28:31], v[124:127], v[48:63]\n"
    "s_mov_b64 exec, 0xff00\n"
    "global_store_dwordx2 v247, v[244:245], s[26:27] sc1\n"
    "s_mov_b64 exec, -1\n"
    "v_lshlrev_b32 v103, 23, v103\n"
    "v_bfe_i32 v104, v204, 16, 1\n"
    "v_lshlrev_b32 v104, 23, v104\n"
    "v_bfe_i32 v105, v204, 17, 1\n"
    "v_lshlrev_b32 v105, 23, v105\n"
    "v_bfe_i32 v106, v204, 18, 1\n"
    "s_waitcnt lgkmcnt(2)\n"
    "v_mfma_f32_32x32x16_bf16 v[32:47], a[32:35], v[112:115], v[32:47]\n"
    "ds_read_b64_tr_b16 v[120:121], v236 offset:16384\n"
    "ds_read_b64_tr_b16 v[122:123], v236 offset:16896\n"
    "ds_read_b64_tr_b16 v[124:125], v254 offset:16384\n"
    "ds_read_b64_tr_b16 v[126:127], v254 offset:16896\n"
    "v_mfma_f32_32x32x16_bf16 v[48:63], a[36:39], v[112:115], v[48:63]\n"
    "s_add_u32 s33, s33, 128\n"
    "s_and_b32 s33, s33, 0x3ff\n"
    "s_mul_i32 s34, s33, 1536\n"
    "s_add_u32 m0, s61, 0\n"
    "s_add_u32 s35, s34, s59\n"
    "buffer_load_dwordx4 v224, s[36:39], s35 offen lds\n"
    "buffer_load_dwordx4 v225, s[36:39], s35 offen offset:1024 lds\n"
    "v_lshlrev_b32 v106, 23, v106\n"
    "s_waitcnt lgkmcnt(4)\n"
    "v_mfma_f32_32x32x16_bf16 v[32:47], a[40:43], v[116:119], v[32:47]\n"
    "v_bfe_i32 v107, v204, 19, 1\n"
    "v_lshlrev_b32 v107, 23, v107\n"
    "v_bfe_i32 v108, v204, 24, 1\n"
    "v_lshlrev_b32 v108, 23, v108\n"
    "v_mfma_f32_32x32x16_bf16 v[48:63], a[44:47], v[116:119], v[48:63]\n"
    "v_bfe_i32 v109, v204, 25, 1\n"
    "ds_write_b32 v228, v64\n"
    "ds_write_b32 v228, v80 offset:128\n"
    "s_waitcnt vmcnt(10) lgkmcnt(0)\n"
    "s_barrier\n"
    "v_mfma_f32_32x32x16_bf16 v[32:47], a[48:51], v[120:123], v[32:47]\n"
    "ds_read_b64_tr_b16 v[112:113], v234 offset:32768\n"
    "ds_read_b64_tr_b16 v[114:115], v234 offset:33280\n"
    "ds_read_b64_tr_b16 v[116:117], v235 offset:32768\n"
    "v_mfma_f32_32x32x16_bf16 v[48:63], a[52:55], v[120:123], v[48:63]\n"
    "ds_read_b64_tr_b16 v[118:119], v235 offset:33280\n"
    "buffer_load_dwordx4 v226, s[36:39], s35 offen offset:2048 lds\n"
    "buffer_load_dwordx4 v227, s[36:39], s35 offen offset:3072 lds\n"
    "v_mfma_f32_32x32x16_bf16 v[32:47], a[56:59], v[124:127], v[32:47]\n"
    "v_lshlrev_b32 v109, 23, v109\n"
    "v_bfe_i32 v110, v204, 26, 1\n"
    "v_lshlrev_b32 v110, 23, v110\n"
    "v_mfma_f32_32x32x16_bf16 v[48:63], a[60:63], v[124:127], v[48:63]\n"
    "v_bfe_i32 v111, v204, 27, 1\n"
    "v_lshlrev_b32 v111, 23, v111\n"
    "v_bfe_i32 v128, v207, 0, 1\n"
    "s_waitcnt lgkmcnt(2)\n"
    "v_mfma_f32_32x32x16_bf16 v[32:47], a[64:67], v[112:115], v[32:47]\n"
    "ds_read_b64_tr_b16 v[120:121], v236 offset:32768\n"
    "ds_read_b64_tr_b16 v[122:123], v236 offset:33280\n"
    "ds_read_b64_tr_b16 v[124:125], v254 offset:32768\n"
    "ds_read_b64_tr_b16 v[126:127], v254 offset:33280\n"
    "s_add_u32 s33, s33, 128\n"
    "s_and_b32 s33, s33, 0x3ff\n"
    "s_mul_i32 s34, s33, 1536\n"
    "s_add_u32 m0, s61, 16384\n"
    "s_add_u32 s35, s34, s59\n"
    "buffer_load_dwordx4 v224, s[36:39], s35 offen lds\n"
    "v_mfma_f32_32x32x16_bf16 v[48:63], a[68:71], v[112:115], v[48:63]\n"
    "buffer_load_dwordx4 v225, s[36:39], s35 offen offset:1024 lds\n"
    "v_lshlrev_b32 v128, 23, v128\n"
    "v_bfe_i32 v129, v207, 1, 1\n"
    "v_lshlrev_b32 v129, 23, v129\n"
    "v_bfe_i32 v130, v207, 2, 1\n"
    "s_waitcnt lgkmcnt(4)\n"
    "v_mfma_f32_32x32x16_bf16 v[32:47], a[72:75], v[116:119], v[32:47]\n"
    "v_lshlrev_b32 v130, 23, v130\n"
    "v_bfe_i32 v131, v207, 3, 1\n"
    "ds_read_b32 v216, v229 offset:0\n"
    "ds_read_b32 v217, v229 offset:256\n"
    "ds_read_b32 v218, v229 offset:512\n"
    "v_mfma_f32_32x32x16_bf16 v[48:63], a[76:79], v[116:119], v[48:63]\n"
    "ds_read_b32 v219, v229 offset:768\n"
    "ds_read_b32 v220, v229 offset:128\n"
    "ds_read_b32 v221, v229 offset:384\n"
    "ds_read_b32 v222, v229 offset:640\n"
    "ds_read_b32 v223, v229 offset:896\n"
    "s_waitcnt vmcnt(10) lgkmcnt(0)\n"
    "s_barrier\n"
    "v_mfma_f32_32x32x16_bf16 v[32:47], a[80:83], v[120:123], v[32:47]\n"
    "ds_read_b64_tr_b16 v[112:113], v234 offset:49152\n"
    "ds_read_b64_tr_b16 v[114:115], v234 offset:49664\n"
    "ds_read_b64_tr_b16 v[116:117], v235 offset:49152\n"
    "v_mfma_f32_32x32x16_bf16 v[48:63], a[84:87], v[120:123], v[48:63]\n"
    "ds_read_b64_tr_b16 v[118:119], v235 offset:49664\n"
    "buffer_load_dwordx4 v226, s[36:39], s35 offen offset:2048 lds\n"
    "buffer_load_dwordx4 v227, s[36:39], s35 offen offset:3072 lds\n"
    "v_mfma_f32_32x32x16_bf16 v[32:47], a[88:91], v[124:127], v[32:47]\n"
    "v_lshlrev_b32 v131, 23, v131\n"
    "v_bfe_i32 v132, v207, 8, 1\n"
    "v_lshlrev_b32 v132, 23, v132\n"
    "v_mfma_f32_32x32x16_bf16 v[48:63], a[92:95], v[124:127], v[48:63]\n"
    "v_bfe_i32 v133, v207, 9, 1\n"
    "v_lshlrev_b32 v133, 23, v133\n"
    "v_bfe_i32 v134, v207, 10, 1\n"
    "s_waitcnt lgkmcnt(2)\n"
    "v_mfma_f32_32x32x16_bf16 v[32:47], a[96:99], v[112:115], v[32:47]\n"
    "ds_read_b64_tr_b16 v[120:121], v236 offset:49152\n"
    "ds_read_b64_tr_b16 v[122:123], v236 offset:49664\n"
    "ds_read_b64_tr_b16 v[124:125], v254 offset:49152\n"
    "ds_read_b64_tr_b16 v[126:127], v254 offset:49664\n"
    "s_add_u32 s33, s33, 128\n"
    "s_and_b32 s33, s33, 0x3ff\n"
    "s_mul_i32 s34, s33, 1536\n"
    "s_add_u32 m0, s61, 32768\n"
    "s_add_u32 s35, s34, s59\n"
    "buffer_load_dwordx4 v224, s[36:39], s35 offen lds\n"
    "buffer_load_dwordx4 v225, s[36:39], s35 offen offset:1024 lds\n"
    "v_lshlrev_b32 v134, 23, v134\n"
    "v_mfma_f32_32x32x16_bf16 v[48:63], a[100:103], v[112:115], v[48:63]\n"
    "v_bfe_i32 v135, v207, 11, 1\n"
    "v_lshlrev_b32 v135, 23, v135\n"
    "v_bfe_i32 v136, v207, 16, 1\n"
    "v_lshlrev_b32 v136, 23, v136\n"
    "v_bfe_i32 v137, v207, 17, 1\n"
    "v_add_f32 v216, v216, v217\n"
    "s_waitcnt lgkmcnt(4)\n"
    "v_mfma_f32_32x32x16_bf16 v[32:47], a[104:107], v[116:119], v[32:47]\n"
    "v_add_f32 v218, v218, v219\n"
    "v_add_f32 v216, v216, v218\n"
    "v_mul_f32 v216, 0xbfb8aa3b, v216\n"
    "v_exp_f32 v216, v216\n"
    "v_add_f32 v220, v220, v221\n"
    "v_add_f32 v222, v222, v223\n"
    "v_add_f32 v220, v220, v222\n"
    "v_mfma_f32_32x32x16_bf16 v[48:63], a[108:111], v[116:119], v[48:63]\n"
    "v_mul_f32 v220, 0xbfb8aa3b, v220\n"
    "v_exp_f32 v220, v220\n"
    "v_add_f32 v216, 1.0, v216\n"
    "v_add_f32 v220, 1.0, v220\n"
    "v_rcp_f32 v252, v216\n"
    "v_rcp_f32 v253, v220\n"
    "s_waitcnt vmcnt(6) lgkmcnt(0)\n"
    "s_barrier\n"
    "v_mfma_f32_32x32x16_bf16 v[32:47], a[112:115], v[120:123], v[32:47]\n"
    "ds_read_b64_tr_b16 v[112:113], v234 offset:0\n"
    "ds_read_b64_tr_b16 v[114:115], v234 offset:512\n"
    "ds_read_b64_tr_b16 v[116:117], v235 offset:0\n"
    "v_mfma_f32_32x32x16_bf16 v[48:63], a[116:119], v[120:123], v[48:63]\n"
    "ds_read_b64_tr_b16 v[118:119], v235 offset:512\n"
    "buffer_load_dwordx4 v226, s[36:39], s35 offen offset:2048 lds\n"
    "buffer_load_dwordx4 v227, s[36:39], s35 offen offset:3072 lds\n"
    "v_mfma_f32_32x32x16_bf16 v[32:47], a[120:123], v[124:127], v[32:47]\n"
    "v_lshlrev_b32 v137, 23, v137\n"
    "v_bfe_i32 v138, v207, 18, 1\n"
    "v_lshlrev_b32 v138, 23, v138\n"
    "v_mfma_f32_32x32x16_bf16 v[48:63], a[124:127], v[124:127], v[48:63]\n"
    "v_bfe_i32 v139, v207, 19, 1\n"
    "v_lshlrev_b32 v139, 23, v139\n"
    "v_bfe_i32 v140, v207, 24, 1\n"
    "s_waitcnt lgkmcnt(2)\n"
    "v_mfma_f32_32x32x16_bf16 v[32:47], a[128:131], v[112:115], v[32:47]\n"
    "ds_read_b64_tr_b16 v[120:121], v236 offset:0\n"
    "ds_read_b64_tr_b16 v[122:123], v236 offset:512\n"
    "ds_read_b64_tr_b16 v[124:125], v254 offset:0\n"
    "v_mfma_f32_32x32x16_bf16 v[48:63], a[132:135], v[112:115], v[48:63]\n"
    "ds_read_b64_tr_b16 v[126:127], v254 offset:512\n"
    "s_add_u32 s33, s33, 128\n"
    "s_and_b32 s33, s33, 0x3ff\n"
    "s_mul_i32 s34, s33, 1536\n"
    "s_add_u32 m0, s61, 49152\n"
    "s_add_u32 s35, s34, s59\n"
    "buffer_load_dwordx4 v224, s[36:39], s35 offen lds\n"
    "buffer_load_dwordx4 v225, s[36:39], s35 offen offset:1024 lds\n"
    "s_waitcnt lgkmcnt(4)\n"
    "v_mfma_f32_32x32x16_bf16 v[32:47], a[136:139], v[116:119], v[32:47]\n"
    "v_lshlrev_b32 v140, 23, v140\n"
    "v_bfe_i32 v141, v207, 25, 1\n"
    "v_lshlrev_b32 v141, 23, v141\n"
    "v_mfma_f32_32x32x16_bf16 v[48:63], a[140:143], v[116:119], v[48:63]\n"
    "v_bfe_i32 v142, v207, 26, 1\n"
    "v_lshlrev_b32 v142, 23, v142\n"
    "v_bfe_i32 v143, v207, 27, 1\n"
    "s_waitcnt vmcnt(6) lgkmcnt(0)\n"
    "s_barrier\n"
    "v_mfma_f32_32x32x16_bf16 v[32:47], a[144:147], v[120:123], v[32:47]\n"
    "ds_read_b64_tr_b16 v[112:113], v234 offset:16384\n"
    "ds_read_b64_tr_b16 v[114:115], v234 offset:16896\n"
    "ds_read_b64_tr_b16 v[116:117], v235 offset:16384\n"
    "v_mfma_f32_32x32x16_bf16 v[48:63], a[148:151], v[120:123], v[48:63]\n"
    "ds_read_b64_tr_b16 v[118:119], v235 offset:16896\n"
    "buffer_load_dwordx4 v226, s[36:39], s35 offen offset:2048 lds\n"
    "buffer_load_dwordx4 v227, s[36:39], s35 offen offset:3072 lds\n"
    "v_mfma_f32_32x32x16_bf16 v[32:47], a[152:155], v[124:127], v[32:47]\n"
    "v_lshlrev_b32 v143, 23, v143\n"
    "v_bfe_i32 v144, v206, 16, 1\n"
    "v_lshlrev_b32 v144, 23, v144\n"
    "v_mfma_f32_32x32x16_bf16 v[48:63], a[156:159], v[124:127], v[48:63]\n"
    "v_bfe_i32 v145, v206, 17, 1\n"
    "v_lshlrev_b32 v145, 23, v145\n"
    "v_bfe_i32 v146, v206, 18, 1\n"
    "s_waitcnt lgkmcnt(2)\n"
    "v_mfma_f32_32x32x16_bf16 v[32:47], a[160:163], v[112:115], v[32:47]\n"
    "ds_read_b64_tr_b16 v[120:121], v236 offset:16384\n"
    "ds_read_b64_tr_b16 v[122:123], v236 offset:16896\n"
    "ds_read_b64_tr_b16 v[124:125], v254 offset:16384\n"
    "v_mfma_f32_32x32x16_bf16 v[48:63], a[164:167], v[112:115], v[48:63]\n"
    "ds_read_b64_tr_b16 v[126:127], v254 offset:16896\n"
    "s_add_u32 s33, s33, 128\n"
    "s_and_b32 s33, s33, 0x3ff\n"
    "s_mul_i32 s34, s33, 1536\n"
    "s_add_u32 m0, s61, 0\n"
    "s_add_u32 s35, s34, s60\n"
    "buffer_load_dwordx4 v224, s[36:39], s35 offen lds\n"
    "buffer_load_dwordx4 v225, s[36:39], s35 offen offset:1024 lds\n"
    "s_waitcnt lgkmcnt(4)\n"
    "v_mfma_f32_32x32x16_bf16 v[32:47], a[168:171], v[116:119], v[32:47]\n"
    "v_lshlrev_b32 v146, 23, v146\n"
    "v_bfe_i32 v147, v206, 19, 1\n"
    "v_lshlrev_b32 v147, 23, v147\n"
    "v_mfma_f32_32x32x16_bf16 v[48:63], a[172:175], v[116:119], v[48:63]\n"
    "v_bfe_i32 v148, v206, 24, 1\n"
    "v_lshlrev_b32 v148, 23, v148\n"
    "v_bfe_i32 v149, v206, 25, 1\n"
    "s_waitcnt vmcnt(6) lgkmcnt(0)\n"
    "s_barrier\n"
    "v_mfma_f32_32x32x16_bf16 v[32:47], a[176:179], v[120:123], v[32:47]\n"
    "ds_read_b64_tr_b16 v[112:113], v234 offset:32768\n"
    "ds_read_b64_tr_b16 v[114:115], v234 offset:33280\n"
    "ds_read_b64_tr_b16 v[116:117], v235 offset:32768\n"
    "v_mfma_f32_32x32x16_bf16 v[48:63], a[180:183], v[120:123], v[48:63]\n"
    "ds_read_b64_tr_b16 v[118:119], v235 offset:33280\n"
    "buffer_load_dwordx4 v226, s[36:39], s35 offen offset:2048 lds\n"
    "buffer_load_dwordx4 v227, s[36:39], s35 offen offset:3072 lds\n"
    "v_mfma_f32_32x32x16_bf16 v[32:47], a[184:187], v[124:127], v[32:47]\n"
    "v_lshlrev_b32 v149, 23, v149\n"
    "v_bfe_i32 v150, v206, 26, 1\n"
    "v_lshlrev_b32 v150, 23, v150\n"
    "v_mfma_f32_32x32x16_bf16 v[48:63], a[188:191], v[124:127], v[48:63]\n"
    "v_bfe_i32 v151, v206, 27, 1\n"
    "v_lshlrev_b32 v151, 23, v151\n"
    "s_waitcnt lgkmcnt(2)\n"
    "v_mfma_f32_32x32x16_bf16 v[32:47], a[192:195], v[112:115], v[32:47]\n"
    "ds_read_b64_tr_b16 v[120:121], v236 offset:32768\n"
    "ds_read_b64_tr_b16 v[122:123], v236 offset:33280\n"
    "v_mfma_f32_32x32x16_bf16 v[48:63], a[196:199], v[112:115], v[48:63]\n"
    "ds_read_b64_tr_b16 v[124:125], v254 offset:32768\n"
    "s_waitcnt lgkmcnt(3)\n"
    "v_mfma_f32_32x32x16_bf16 v[32:47], a[200:203], v[116:119], v[32:47]\n"
    "ds_read_b64_tr_b16 v[126:127], v254 offset:33280\n"
    "s_add_u32 s33, s33, 128\n"
    "s_and_b32 s33, s33, 0x3ff\n"
    "s_mul_i32 s34, s33, 1536\n"
    "s_add_u32 m0, s61, 16384\n"
    "s_add_u32 s35, s34, s60\n"
    "buffer_load_dwordx4 v224, s[36:39], s35 offen lds\n"
    "v_mfma_f32_32x32x16_bf16 v[48:63], a[204:207], v[116:119], v[48:63]\n"
    "buffer_load_dwordx4 v225, s[36:39], s35 offen offset:1024 lds\n"
    "s_waitcnt vmcnt(6) lgkmcnt(0)\n"
    "s_barrier\n"
    "v_mfma_f32_32x32x16_bf16 v[32:47], a[208:211], v[120:123], v[32:47]\n"
    "ds_read_b64_tr_b16 v[112:113], v234 offset:49152\n"
    "ds_read_b64_tr_b16 v[114:115], v234 offset:49664\n"
    "v_mfma_f32_32x32x16_bf16 v[48:63], a[212:215], v[120:123], v[48:63]\n"
    "ds_read_b64_tr_b16 v[116:117], v235 offset:49152\n"
    "v_mfma_f32_32x32x16_bf16 v[32:47], a[216:219], v[124:127], v[32:47]\n"
    "ds_read_b64_tr_b16 v[118:119], v235 offset:49664\n"
    "buffer_load_dwordx4 v226, s[36:39], s35 offen offset:2048 lds\n"
    "v_mfma_f32_32x32x16_bf16 v[48:63], a[220:223], v[124:127], v[48:63]\n"
    "buffer_load_dwordx4 v227, s[36:39], s35 offen offset:3072 lds\n"
    "s_waitcnt lgkmcnt(2)\n"
    "v_mfma_f32_32x32x16_bf16 v[32:47], a[224:227], v[112:115], v[32:47]\n"
    "ds_read_b64_tr_b16 v[120:121], v236 offset:49152\n"
    "ds_read_b64_tr_b16 v[122:123], v236 offset:49664\n"
    "v_mfma_f32_32x32x16_bf16 v[48:63], a[228:231], v[112:115], v[48:63]\n"
    "ds_read_b64_tr_b16 v[124:125], v254 offset:49152\n"
    "s_waitcnt lgkmcnt(3)\n"
    "v_mfma_f32_32x32x16_bf16 v[32:47], a[232:235], v[116:119], v[32:47]\n"
    "ds_read_b64_tr_b16 v[126:127], v254 offset:49664\n"
    "s_add_u32 s33, s33, 128\n"
    "s_and_b32 s33, s33, 0x3ff\n"
    "s_mul_i32 s34, s33, 1536\n"
    "s_add_u32 m0, s61, 32768\n"
    "s_add_u32 s35, s34, s60\n"
    "buffer_load_dwordx4 v224, s[36:39], s35 offen lds\n"
    "v_mfma_f32_32x32x16_bf16 v[48:63], a[236:239], v[116:119], v[48:63]\n"
    "buffer_load_dwordx4 v225, s[36:39], s35 offen offset:1024 lds\n"
    "s_waitcnt vmcnt(6) lgkmcnt(0)\n"
    "s_barrier\n"
    "v_mfma_f32_32x32x16_bf16 v[32:47], a[240:243], v[120:123], v[32:47]\n"
    "ds_read_b64_tr_b16 v[112:113], v234 offset:0\n"
    "ds_read_b64_tr_b16 v[114:115], v234 offset:512\n"
    "v_mfma_f32_32x32x16_bf16 v[48:63], a[244:247], v[120:123], v[48:63]\n"
    "ds_read_b64_tr_b16 v[116:117], v235 offset:0\n"
    "v_mfma_f32_32x32x16_bf16 v[32:47], a[248:251], v[124:127], v[32:47]\n"
    "ds_read_b64_tr_b16 v[118:119], v235 offset:512\n"
    "buffer_load_dwordx4 v226, s[36:39], s35 offen offset:2048 lds\n"
    "v_mfma_f32_32x32x16_bf16 v[48:63], a[252:255], v[124:127], v[48:63]\n"
    "buffer_load_dwordx4 v227, s[36:39], s35 offen offset:3072 lds\n"
    "s_waitcnt lgkmcnt(2)\n"
    "v_mfma_f32_32x32x16_bf16 v[64:79], v[112:115], a[0:3], 0\n"
    "ds_read_b64_tr_b16 v[120:121], v236 offset:0\n"
    "ds_read_b64_tr_b16 v[122:123], v236 offset:512\n"
    "ds_read_b64_tr_b16 v[124:125], v254 offset:0\n"
    "ds_read_b64_tr_b16 v[126:127], v254 offset:512\n"
    "v_mfma_f32_32x32x16_bf16 v[80:95], v[112:115], a[4:7], 0\n"
    "s_add_u32 s33, s33, 128\n"
    "s_and_b32 s33, s33, 0x3ff\n"
    "s_mul_i32 s34, s33, 1536\n"
    "s_add_u32 m0, s61, 49152\n"
    "s_add_u32 s35, s34, s60\n"
    "buffer_load_dwordx4 v224, s[36:39], s35 offen lds\n"
    "buffer_load_dwordx4 v225, s[36:39], s35 offen offset:1024 lds\n"
    "v_cvt_pk_bf16_f32 v168, v32, v33\n"
    "s_waitcnt lgkmcnt(4)\n"
    "v_mfma_f32_32x32x16_bf16 v[64:79], v[116:119], a[8:11], v[64:79]\n"
    "v_cvt_pk_bf16_f32 v169, v34, v35\n"
    "v_cvt_pk_bf16_f32 v170, v36, v37\n"
    "v_cvt_pk_bf16_f32 v171, v38, v39\n"
    "v_cvt_pk_bf16_f32 v172, v40, v41\n"
    "v_mfma_f32_32x32x16_bf16 v[80:95], v[116:119], a[12:15], v[80:95]\n"
    "v_cvt_pk_bf16_f32 v173, v42, v43\n"
    "v_cvt_pk_bf16_f32 v174, v44, v45\n"
    "v_cvt_pk_bf16_f32 v175, v46, v47\n"
    "s_waitcnt vmcnt(6) lgkmcnt(0)\n"
    "s_barrier\n"
    "v_mfma_f32_32x32x16_bf16 v[64:79], v[120:123], a[16:19], v[64:79]\n"
    "ds_read_b64_tr_b16 v[112:113], v234 offset:16384\n"
    "ds_read_b64_tr_b16 v[114:115], v234 offset:16896\n"
    "ds_read_b64_tr_b16 v[116:117], v235 offset:16384\n"
    "ds_read_b64_tr_b16 v[118:119], v235 offset:16896\n"
    "v_mfma_f32_32x32x16_bf16 v[80:95], v[120:123], a[20:23], v[80:95]\n"
    "buffer_load_dwordx4 v226, s[36:39], s35 offen offset:2048 lds\n"
    "buffer_load_dwordx4 v227, s[36:39], s35 offen offset:3072 lds\n"
    "v_cvt_pk_bf16_f32 v176, v48, v49\n"
    "v_cvt_pk_bf16_f32 v177, v50, v51\n"
    "v_mfma_f32_32x32x16_bf16 v[64:79], v[124:127], a[24:27], v[64:79]\n"
    "v_cvt_pk_bf16_f32 v178, v52, v53\n"
    "v_cvt_pk_bf16_f32 v179, v54, v55\n"
    "v_cvt_pk_bf16_f32 v180, v56, v57\n"
    "v_cvt_pk_bf16_f32 v181, v58, v59\n"
    "v_mfma_f32_32x32x16_bf16 v[80:95], v[124:127], a[28:31], v[80:95]\n"
    "v_cvt_pk_bf16_f32 v182, v60, v61\n"
    "v_cvt_pk_bf16_f32 v183, v62, v63\n"
    "s_mov_b64 exec, s[68:69]\n"
    "global_store_dwordx4 v237, v[168:171], s[84:85] offset:0 sc1\n"
    "global_store_dwordx4 v237, v[172:175], s[84:85] offset:1024 sc1\n"
    "global_store_dwordx4 v237, v[176:179], s[84:85] offset:2048 sc1\n"
    "s_mov_b64 exec, -1\n"
    "global_store_dwordx4 v237, v[180:183], s[84:85] offset:3072 sc1\n"
    "s_waitcnt lgkmcnt(2)\n"
    "v_mfma_f32_32x32x16_bf16 v[64:79], v[112:115], a[32:35], v[64:79]\n"
    "ds_read_b64_tr_b16 v[120:121], v236 offset:16384\n"
    "ds_read_b64_tr_b16 v[122:123], v236 offset:16896\n"
    "v_mfma_f32_32x32x16_bf16 v[80:95], v[112:115], a[36:39], v[80:95]\n"
    "ds_read_b64_tr_b16 v[124:125], v254 offset:16384\n"
    "s_waitcnt lgkmcnt(3)\n"
    "v_mfma_f32_32x32x16_bf16 v[64:79], v[116:119], a[40:43], v[64:79]\n"
    "ds_read_b64_tr_b16 v[126:127], v254 offset:16896\n"
    "s_add_u32 s33, s33, 128\n"
    "s_and_b32 s33, s33, 0x3ff\n"
    "s_mul_i32 s34, s33, 1536\n"
    "s_add_u32 m0, s61, 0\n"
    "s_add_u32 s35, s34, s60\n"
    "buffer_load_dwordx4 v224, s[36:39], s35 offen lds\n"
    "v_mfma_f32_32x32x16_bf16 v[80:95], v[116:119], a[44:47], v[80:95]\n"
    "buffer_load_dwordx4 v225, s[36:39], s35 offen offset:1024 lds\n"
    "s_waitcnt vmcnt(10) lgkmcnt(0)\n"
    "s_barrier\n"
    "v_mfma_f32_32x32x16_bf16 v[64:79], v[120:123], a[48:51], v[64:79]\n"
    "ds_read_b64_tr_b16 v[112:113], v234 offset:32768\n"
    "ds_read_b64_tr_b16 v[114:115], v234 offset:33280\n"
    "v_mfma_f32_32x32x16_bf16 v[80:95], v[120:123], a[52:55], v[80:95]\n"
    "ds_read_b64_tr_b16 v[116:117], v235 offset:32768\n"
    "v_mfma_f32_32x32x16_bf16 v[64:79], v[124:127], a[56:59], v[64:79]\n"
    "ds_read_b64_tr_b16 v[118:119], v235 offset:33280\n"
    "buffer_load_dwordx4 v226, s[36:39], s35 offen offset:2048 lds\n"
    "v_mfma_f32_32x32x16_bf16 v[80:95], v[124:127], a[60:63], v[80:95]\n"
    "buffer_load_dwordx4 v227, s[36:39], s35 offen offset:3072 lds\n"
    "s_waitcnt lgkmcnt(2)\n"
    "v_mfma_f32_32x32x16_bf16 v[64:79], v[112:115], a[64:67], v[64:79]\n"
    "ds_read_b64_tr_b16 v[120:121], v236 offset:32768\n"
    "ds_read_b64_tr_b16 v[122:123], v236 offset:33280\n"
    "ds_read_b64_tr_b16 v[124:125], v254 offset:32768\n"
    "ds_read_b64_tr_b16 v[126:127], v254 offset:33280\n"
    "s_add_u32 s33, s33, 128\n"
    "s_and_b32 s33, s33, 0x3ff\n"
    "s_mul_i32 s34, s33, 1536\n"
    "s_add_u32 m0, s61, 16384\n"
    "s_add_u32 s35, s34, s60\n"
    "buffer_load_dwordx4 v224, s[36:39], s35 offen lds\n"
    "buffer_load_dwordx4 v225, s[36:39], s35 offen offset:1024 lds\n"
    "v_mfma_f32_32x32x16_bf16 v[80:95], v[112:115], a[68:71], v[80:95]\n"
    "v_cndmask_b32 v184, v168, v239, s[68:69]\n"
    "v_cndmask_b32 v168, v239, v168, s[68:69]\n"
    "v_cndmask_b32 v185, v169, v239, s[68:69]\n"
    "v_cndmask_b32 v169, v239, v169, s[68:69]\n"
    "v_cndmask_b32 v186, v170, v239, s[68:69]\n"
    "s_waitcnt lgkmcnt(4)\n"
    "v_mfma_f32_32x32x16_bf16 v[64:79], v[116:119], a[72:75], v[64:79]\n"
    "v_cndmask_b32 v170, v239, v170, s[68:69]\n"
    "v_cndmask_b32 v187, v171, v239, s[68:69]\n"
    "v_cndmask_b32 v171, v239, v171, s[68:69]\n"
    "v_cndmask_b32 v188, v172, v239, s[68:69]\n"
    "v_cndmask_b32 v172, v239, v172, s[68:69]\n"
    "v_cndmask_b32 v189, v173, v239, s[68:69]\n"
    "v_mfma_f32_32x32x16_bf16 v[80:95], v[116:119], a[76:79], v[80:95]\n"
    "v_cndmask_b32 v173, v239, v173, s[68:69]\n"
    "v_cndmask_b32 v190, v174, v239, s[68:69]\n"
    "v_cndmask_b32 v174, v239, v174, s[68:69]\n"
    "v_cndmask_b32 v191, v175, v239, s[68:69]\n"
    "v_cndmask_b32 v175, v239, v175, s[68:69]\n"
    "s_waitcnt vmcnt(10) lgkmcnt(0)\n"
    "s_barrier\n"
    "v_mfma_f32_32x32x16_bf16 v[64:79], v[120:123], a[80:83], v[64:79]\n"
    "ds_read_b64_tr_b16 v[112:113], v234 offset:49152\n"
    "ds_read_b64_tr_b16 v[114:115], v234 offset:49664\n"
    "ds_read_b64_tr_b16 v[116:117], v235 offset:49152\n"
    "ds_read_b64_tr_b16 v[118:119], v235 offset:49664\n"
    "buffer_load_dwordx4 v226, s[36:39], s35 offen offset:2048 lds\n"
    "buffer_load_dwordx4 v227, s[36:39], s35 offen offset:3072 lds\n"
    "v_mfma_f32_32x32x16_bf16 v[80:95], v[120:123], a[84:87], v[80:95]\n"
    "v_cndmask_b32 v192, v176, v239, s[68:69]\n"
    "v_cndmask_b32 v176, v239, v176, s[68:69]\n"
    "v_cndmask_b32 v193, v177, v239, s[68:69]\n"
    "v_cndmask_b32 v177, v239, v177, s[68:69]\n"
    "v_cndmask_b32 v194, v178, v239, s[68:69]\n"
    "v_mfma_f32_32x32x16_bf16 v[64:79], v[124:127], a[88:91], v[64:79]\n"
    "v_cndmask_b32 v178, v239, v178, s[68:69]\n"
    "v_cndmask_b32 v195, v179, v239, s[68:69]\n"
    "v_cndmask_b32 v179, v239, v179, s[68:69]\n"
    "v_cndmask_b32 v196, v180, v239, s[68:69]\n"
    "v_cndmask_b32 v180, v239, v180, s[68:69]\n"
    "v_cndmask_b32 v197, v181, v239, s[68:69]\n"
    "v_mfma_f32_32x32x16_bf16 v[80:95], v[124:127], a[92:95], v[80:95]\n"
    "v_cndmask_b32 v181, v239, v181, s[68:69]\n"
    "v_cndmask_b32 v198, v182, v239, s[68:69]\n"
    "v_cndmask_b32 v182, v239, v182, s[68:69]\n"
    "v_cndmask_b32 v199, v183, v239, s[68:69]\n"
    "v_cndmask_b32 v183, v239, v183, s[68:69]\n"
    "s_waitcnt lgkmcnt(2)\n"
    "v_mfma_f32_32x32x16_bf16 v[64:79], v[112:115], a[96:99], v[64:79]\n"
    "ds_read_b64_tr_b16 v[120:121], v236 offset:49152\n"
    "ds_read_b64_tr_b16 v[122:123], v236 offset:49664\n"
    "ds_read_b64_tr_b16 v[124:125], v254 offset:49152\n"
    "ds_read_b64_tr_b16 v[126:127], v254 offset:49664\n"
    "s_add_u32 s33, s33, 128\n"
    "s_and_b32 s33, s33, 0x3ff\n"
    "s_mul_i32 s34, s33, 1536\n"
    "s_add_u32 m0, s61, 32768\n"
    "s_add_u32 s35, s34, s60\n"
    "buffer_load_dwordx4 v224, s[36:39], s35 offen lds\n"
    "v_mfma_f32_32x32x16_bf16 v[80:95], v[112:115], a[100:103], v[80:95]\n"
    "buffer_load_dwordx4 v225, s[36:39], s35 offen offset:1024 lds\n"
    "v_accvgpr_write_b32 a32, s80\n"
    "v_accvgpr_write_b32 a33, s80\n"
    "v_accvgpr_write_b32 a34, s80\n"
    "s_waitcnt lgkmcnt(4)\n"
    "v_mfma_f32_32x32x16_bf16 v[64:79], v[116:119], a[104:107], v[64:79]\n"
    "v_accvgpr_write_b32 a35, s80\n"
    "v_accvgpr_write_b32 a36, s80\n"
    "v_accvgpr_write_b32 a37, s80\n"
    "v_accvgpr_write_b32 a38, s80\n"
    "v_accvgpr_write_b32 a39, s80\n"
    "v_mfma_f32_32x32x16_bf16 v[80:95], v[116:119], a[108:111], v[80:95]\n"
    "v_accvgpr_write_b32 a40, s80\n"
    "v_accvgpr_write_b32 a41, s80\n"
    "v_accvgpr_write_b32 a42, s80\n"
    "v_accvgpr_write_b32 a43, s80\n"
    "s_waitcnt vmcnt(6) lgkmcnt(0)\n"
    "s_barrier\n"
    "v_mfma_f32_32x32x16_bf16 v[64:79], v[120:123], a[112:115], v[64:79]\n"
    "ds_read_b64_tr_b16 v[112:113], v234 offset:0\n"
    "ds_read_b64_tr_b16 v[114:115], v234 offset:512\n"
    "ds_read_b64_tr_b16 v[116:117], v235 offset:0\n"
    "ds_read_b64_tr_b16 v[118:119], v235 offset:512\n"
    "buffer_load_dwordx4 v226, s[36:39], s35 offen offset:2048 lds\n"
    "v_mfma_f32_32x32x16_bf16 v[80:95], v[120:123], a[116:119], v[80:95]\n"
    "buffer_load_dwordx4 v227, s[36:39], s35 offen offset:3072 lds\n"
    "v_accvgpr_write_b32 a44, s80\n"
    "v_accvgpr_write_b32 a45, s80\n"
    "v_accvgpr_write_b32 a46, s80\n"
    "v_mfma_f32_32x32x16_bf16 v[64:79], v[124:127], a[120:123], v[64:79]\n"
    "v_accvgpr_write_b32 a47, s80\n"
    "v_accvgpr_write_b32 a48, s81\n"
    "v_accvgpr_write_b32 a49, s81\n"
    "v_accvgpr_write_b32 a50, s81\n"
    "v_accvgpr_write_b32 a51, s81\n"
    "v_mfma_f32_32x32x16_bf16 v[80:95], v[124:127], a[124:127], v[80:95]\n"
    "v_accvgpr_write_b32 a52, s81\n"
    "v_accvgpr_write_b32 a53, s81\n"
    "v_accvgpr_write_b32 a54, s81\n"
    "v_accvgpr_write_b32 a55, s81\n"
    "s_waitcnt lgkmcnt(2)\n"
    "v_mfma_f32_32x32x16_bf16 v[64:79], v[112:115], a[128:131], v[64:79]\n"
    "ds_read_b64_tr_b16 v[120:121], v236 offset:0\n"
    "ds_read_b64_tr_b16 v[122:123], v236 offset:512\n"
    "ds_read_b64_tr_b16 v[124:125], v254 offset:0\n"
    "ds_read_b64_tr_b16 v[126:127], v254 offset:512\n"
    "s_add_u32 s33, s33, 128\n"
    "s_and_b32 s33, s33, 0x3ff\n"
    "s_mul_i32 s34, s33, 1536\n"
    "s_add_u32 m0, s61, 49152\n"
    "s_add_u32 s35, s34, s60\n"
    "buffer_load_dwordx4 v224, s[36:39], s35 offen lds\n"
    "v_mfma_f32_32x32x16_bf16 v[80:95], v[112:115], a[132:135], v[80:95]\n"
    "buffer_load_dwordx4 v225, s[36:39], s35 offen offset:1024 lds\n"
    "v_accvgpr_write_b32 a56, s81\n"
    "v_accvgpr_write_b32 a57, s81\n"
    "v_accvgpr_write_b32 a58, s81\n"
    "s_waitcnt lgkmcnt(4)\n"
    "v_mfma_f32_32x32x16_bf16 v[64:79], v[116:119], a[136:139], v[64:79]\n"
    "v_accvgpr_write_b32 a59, s81\n"
    "v_accvgpr_write_b32 a60, s81\n"
    "v_accvgpr_write_b32 a61, s81\n"
    "v_accvgpr_write_b32 a62, s81\n"
    "v_accvgpr_write_b32 a63, s81\n"
    "v_mfma_f32_32x32x16_bf16 v[80:95], v[116:119], a[140:143], v[80:95]\n"
    "v_accvgpr_write_b32 a64, s82\n"
    "v_accvgpr_write_b32 a65, s82\n"
    "v_accvgpr_write_b32 a66, s82\n"
    "v_accvgpr_write_b32 a67, s82\n"
    "s_waitcnt vmcnt(6) lgkmcnt(0)\n"
    "s_barrier\n"
    "v_mfma_f32_32x32x16_bf16 v[64:79], v[120:123], a[144:147], v[64:79]\n"
    "ds_read_b64_tr_b16 v[112:113], v234 offset:16384\n"
    "ds_read_b64_tr_b16 v[114:115], v234 offset:16896\n"
    "ds_read_b64_tr_b16 v[116:117], v235 offset:16384\n"
    "ds_read_b64_tr_b16 v[118:119], v235 offset:16896\n"
    "buffer_load_dwordx4 v226, s[36:39], s35 offen offset:2048 lds\n"
    "buffer_load_dwordx4 v227, s[36:39], s35 offen offset:3072 lds\n"
    "v_mfma_f32_32x32x16_bf16 v[80:95], v[120:123], a[148:151], v[80:95]\n"
    "s_mov_b64 exec, 15\n"
    "global_store_dwordx2 v247, v[244:245], s[26:27] sc1\n"
    "s_mov_b64 exec, -1\n"
    "v_accvgpr_write_b32 a68, s82\n"
    "v_accvgpr_write_b32 a69, s82\n"
    "v_accvgpr_write_b32 a70, s82\n"
    "v_accvgpr_write_b32 a71, s82\n"
    "v_accvgpr_write_b32 a72, s82\n"
    "v_mfma_f32_32x32x16_bf16 v[64:79], v[124:127], a[152:155], v[64:79]\n"
    "v_accvgpr_write_b32 a73, s82\n"
    "v_accvgpr_write_b32 a74, s82\n"
    "v_accvgpr_write_b32 a75, s82\n"
    "v_accvgpr_write_b32 a76, s82\n"
    "v_accvgpr_write_b32 a77, s82\n"
    "v_accvgpr_write_b32 a78, s82\n"
    "v_mfma_f32_32x32x16_bf16 v[80:95], v[124:127], a[156:159], v[80:95]\n"
    "v_accvgpr_write_b32 a79, s82\n"
    "s_mov_b32 s9, 0x3eb8aa3b\n"
    "s_add_u32 m0, s21, 32768\n"
    "s_lshl4_add_u32 s25, s22, s24\n"
    "buffer_load_dwordx4 v230, s[44:47], s25 offen lds\n"
    "buffer_load_dwordx4 v231, s[44:47], s25 offen offset:1024 lds\n"
    "buffer_load_dwordx4 v232, s[44:47], s25 offen offset:2048 lds\n"
    "buffer_load_dwordx4 v233, s[44:47], s25 offen offset:3072 lds\n"
    "s_waitcnt lgkmcnt(2)\n"
    "v_mfma_f32_32x32x16_bf16 v[64:79], v[112:115], a[160:163], v[64:79]\n"
    "ds_read_b64_tr_b16 v[120:121], v236 offset:16384\n"
    "ds_read_b64_tr_b16 v[122:123], v236 offset:16896\n"
    "ds_read_b64_tr_b16 v[124:125], v254 offset:16384\n"
    "ds_read_b64_tr_b16 v[126:127], v254 offset:16896\n"
    "v_accvgpr_write_b32 a80, s83\n"
    "v_mfma_f32_32x32x16_bf16 v[80:95], v[112:115], a[164:167], v[80:95]\n"
    "v_accvgpr_write_b32 a81, s83\n"
    "v_accvgpr_write_b32 a82, s83\n"
    "v_accvgpr_write_b32 a83, s83\n"
    "v_accvgpr_write_b32 a84, s83\n"
    "v_accvgpr_write_b32 a85, s83\n"
    "s_waitcnt lgkmcnt(4)\n"
    "v_mfma_f32_32x32x16_bf16 v[64:79], v[116:119], a[168:171], v[64:79]\n"
    "v_accvgpr_write_b32 a86, s83\n"
    "v_accvgpr_write_b32 a87, s83\n"
    "v_accvgpr_write_b32 a88, s83\n"
    "v_accvgpr_write_b32 a89, s83\n"
    "v_accvgpr_write_b32 a90, s83\n"
    "v_mfma_f32_32x32x16_bf16 v[80:95], v[116:119], a[172:175], v[80:95]\n"
    "v_accvgpr_write_b32 a91, s83\n"
    "s_add_u32 m0, s21, 49152\n"
    "s_lshl4_add_u32 s25, s23, s24\n"
    "buffer_load_dwordx4 v230, s[44:47], s25 offen lds\n"
    "buffer_load_dwordx4 v231, s[44:47], s25 offen offset:1024 lds\n"
    "buffer_load_dwordx4 v232, s[44:47], s25 offen offset:2048 lds\n"
    "buffer_load_dwordx4 v233, s[44:47], s25 offen offset:3072 lds\n"
    "s_waitcnt vmcnt(13) lgkmcnt(0)\n"
    "s_barrier\n"
    "v_mfma_f32_32x32x16_bf16 v[64:79], v[120:123], a[176:179], v[64:79]\n"
    "ds_read_b64_tr_b16 v[112:113], v234 offset:32768\n"
    "ds_read_b64_tr_b16 v[114:115], v234 offset:33280\n"
    "ds_read_b64_tr_b16 v[116:117], v235 offset:32768\n"
    "ds_read_b64_tr_b16 v[118:119], v235 offset:33280\n"
    "v_mfma_f32_32x32x16_bf16 v[80:95], v[120:123], a[180:183], v[80:95]\n"
    "v_accvgpr_write_b32 a92, s83\n"
    "v_accvgpr_write_b32 a93, s83\n"
    "v_accvgpr_write_b32 a94, s83\n"
    "v_accvgpr_write_b32 a95, s83\n"
    "v_mfma_f32_32x32x16_bf16 v[64:79], v[124:127], a[184:187], v[64:79]\n"
    "v_accvgpr_write_b32 a100, s83\n"
    "v_accvgpr_write_b32 a101, s83\n"
    "v_accvgpr_write_b32 a102, s83\n"
    "v_accvgpr_write_b32 a103, s83\n"
    "v_mfma_f32_32x32x16_bf16 v[80:95], v[124:127], a[188:191], v[80:95]\n"
    "s_add_u32 m0, s21, 65536\n"
    "s_lshl4_add_u32 s25, s18, s24\n"
    "buffer_load_dwordx4 v230, s[48:51], s25 offen lds\n"
    "buffer_load_dwordx4 v231, s[48:51], s25 offen offset:1024 lds\n"
    "buffer_load_dwordx4 v232, s[48:51], s25 offen offset:2048 lds\n"
    "buffer_load_dwordx4 v233, s[48:51], s25 offen offset:3072 lds\n"
    "s_waitcnt lgkmcnt(2)\n"
    "v_mfma_f32_32x32x16_bf16 v[64:79], v[112:115], a[192:195], v[64:79]\n"
    "global_load_dwordx2 v[248:249], v250, s[26:27] sc1\n"
    "ds_read_b64_tr_b16 v[120:121], v236 offset:32768\n"
    "ds_read_b64_tr_b16 v[122:123], v236 offset:33280\n"
    "ds_read_b64_tr_b16 v[124:125], v254 offset:32768\n"
    "ds_read_b64_tr_b16 v[126:127], v254 offset:33280\n"
    "v_lshrrev_b32 v200, 4, v237\n"
    "v_mfma_f32_32x32x16_bf16 v[80:95], v[112:115], a[196:199], v[80:95]\n"
    "v_bfe_u32 v201, v200, 2, 2\n"
    "v_lshrrev_b32 v202, 5, v200\n"
    "v_lshl_add_u32 v202, v202, 2, v201\n"
    "v_lshrrev_b32 v201, 1, v201\n"
    "v_lshlrev_b32 v202, 7, v202\n"
    "s_waitcnt lgkmcnt(4)\n"
    "v_mfma_f32_32x32x16_bf16 v[64:79], v[116:119], a[200:203], v[64:79]\n"
    "v_lshl_add_u32 v202, v201, 6, v202\n"
    "v_bfe_u32 v201, v200, 4, 1\n"
    "v_lshl_add_u32 v202, v201, 5, v202\n"
    "v_and_b32 v201, 3, v200\n"
    "v_lshl_add_u32 v202, v201, 3, v202\n"
    "v_add_u32 v241, s21, v202\n"
    "v_mfma_f32_32x32x16_bf16 v[80:95], v[116:119], a[204:207], v[80:95]\n"
    "v_xor_b32 v242, 64, v241\n"
    "s_add_u32 m0, s21, 81920\n"
    "s_lshl4_add_u32 s25, s19, s24\n"
    "buffer_load_dwordx4 v230, s[48:51], s25 offen lds\n"
    "buffer_load_dwordx4 v231, s[48:51], s25 offen offset:1024 lds\n"
    "buffer_load_dwordx4 v232, s[48:51], s25 offen offset:2048 lds\n"
    "buffer_load_dwordx4 v233, s[48:51], s25 offen offset:3072 lds\n"
    "s_waitcnt vmcnt(18) lgkmcnt(0)\n"
    "s_barrier\n"
    "v_mfma_f32_32x32x16_bf16 v[64:79], v[120:123], a[208:211], v[64:79]\n"
    "ds_read_b64_tr_b16 v[112:113], v234 offset:49152\n"
    "ds_read_b64_tr_b16 v[114:115], v234 offset:49664\n"
    "v_mfma_f32_32x32x16_bf16 v[80:95], v[120:123], a[212:215], v[80:95]\n"
    "ds_read_b64_tr_b16 v[116:117], v235 offset:49152\n"
    "ds_read_b64_tr_b16 v[118:119], v235 offset:49664\n"
    "v_mfma_f32_32x32x16_bf16 v[64:79], v[124:127], a[216:219], v[64:79]\n"
    "s_add_u32 m0, s21, 98304\n"
    "s_lshl4_add_u32 s25, s22, s24\n"
    "buffer_load_dwordx4 v230, s[48:51], s25 offen lds\n"
    "buffer_load_dwordx4 v231, s[48:51], s25 offen offset:1024 lds\n"
    "v_mfma_f32_32x32x16_bf16 v[80:95], v[124:127], a[220:223], v[80:95]\n"
    "buffer_load_dwordx4 v232, s[48:51], s25 offen offset:2048 lds\n"
    "buffer_load_dwordx4 v233, s[48:51], s25 offen offset:3072 lds\n"
    "s_waitcnt lgkmcnt(2)\n"
    "v_mfma_f32_32x32x16_bf16 v[64:79], v[112:115], a[224:227], v[64:79]\n"
    "ds_read_b64_tr_b16 v[120:121], v236 offset:49152\n"
    "ds_read_b64_tr_b16 v[122:123], v236 offset:49664\n"
    "v_mfma_f32_32x32x16_bf16 v[80:95], v[112:115], a[228:231], v[80:95]\n"
    "ds_read_b64_tr_b16 v[124:125], v254 offset:49152\n"
    "ds_read_b64_tr_b16 v[126:127], v254 offset:49664\n"
    "s_waitcnt lgkmcnt(4)\n"
    "v_mfma_f32_32x32x16_bf16 v[64:79], v[116:119], a[232:235], v[64:79]\n"
    "s_add_u32 m0, s21, 114688\n"
    "s_lshl4_add_u32 s25, s23, s24\n"
    "buffer_load_dwordx4 v230, s[48:51], s25 offen lds\n"
    "buffer_load_dwordx4 v231, s[48:51], s25 offen offset:1024 lds\n"
    "v_mfma_f32_32x32x16_bf16 v[80:95], v[116:119], a[236:239], v[80:95]\n"
    "buffer_load_dwordx4 v232, s[48:51], s25 offen offset:2048 lds\n"
    "buffer_load_dwordx4 v233, s[48:51], s25 offen offset:3072 lds\n"
    "s_waitcnt lgkmcnt(2)\n"
    "v_mfma_f32_32x32x16_bf16 v[64:79], v[120:123], a[240:243], v[64:79]\n"
    "s_waitcnt lgkmcnt(0)\n"
    "v_mfma_f32_32x32x16_bf16 v[64:79], v[124:127], a[248:251], v[64:79]\n"
    "v_mfma_f32_32x32x16_bf16 v[80:95], v[120:123], a[244:247], v[80:95]\n"
    "v_mfma_f32_32x32x16_bf16 v[80:95], v[124:127], a[252:255], v[80:95]\n"
    "s_nop 9\n"
    "v_mul_f32 v64, s9, v64\n"
    "v_mul_f32 v65, s9, v65\n"
    "v_mul_f32 v66, s9, v66\n"
    "v_mul_f32 v67, s9, v67\n"
    "v_mul_f32 v68, s9, v68\n"
    "v_mul_f32 v69, s9, v69\n"
    "v_mul_f32 v70, s9, v70\n"
    "v_mul_f32 v71, s9, v71\n"
    "v_mul_f32 v72, s9, v72\n"
    "v_mul_f32 v73, s9, v73\n"
    "v_mul_f32 v74, s9, v74\n"
    "v_mul_f32 v75, s9, v75\n"
    "v_mul_f32 v76, s9, v76\n"
    "v_mul_f32 v77, s9, v77\n"
    "v_mul_f32 v78, s9, v78\n"
    "v_mul_f32 v79, s9, v79\n"
    "v_cvt_pk_bf16_f32 v200, v64, v65\n"
    "v_cvt_pk_bf16_f32 v201, v66, v67\n"
    "v_cvt_pk_bf16_f32 v202, v68, v69\n"
    "v_cvt_pk_bf16_f32 v203, v70, v71\n"
    "v_cvt_pk_bf16_f32 v208, v72, v73\n"
    "v_cvt_pk_bf16_f32 v209, v74, v75\n"
    "v_cvt_pk_bf16_f32 v210, v76, v77\n"
    "v_cvt_pk_bf16_f32 v211, v78, v79\n"
    "v_mfma_f32_32x32x16_bf16 v[64:79], v[152:155], v[200:203], v[96:111]\n"
    "v_mul_f32 v80, s9, v80\n"
    "v_mul_f32 v81, s9, v81\n"
    "v_mul_f32 v82, s9, v82\n"
    "v_mul_f32 v83, s9, v83\n"
    "v_mul_f32 v84, s9, v84\n"
    "v_mul_f32 v85, s9, v85\n"
    "v_mul_f32 v86, s9, v86\n"
    "v_mul_f32 v87, s9, v87\n"
    "v_mul_f32 v88, s9, v88\n"
    "v_mul_f32 v89, s9, v89\n"
    "v_mul_f32 v90, s9, v90\n"
    "v_mul_f32 v91, s9, v91\n"
    "v_mul_f32 v92, s9, v92\n"
    "v_mul_f32 v93, s9, v93\n"
    "v_mul_f32 v94, s9, v94\n"
    "v_mul_f32 v95, s9, v95\n"
    "v_cvt_pk_bf16_f32 v204, v80, v81\n"
    "v_cvt_pk_bf16_f32 v205, v82, v83\n"
    "v_cvt_pk_bf16_f32 v206, v84, v85\n"
    "v_cvt_pk_bf16_f32 v207, v86, v87\n"
    "v_cvt_pk_bf16_f32 v212, v88, v89\n"
    "v_cvt_pk_bf16_f32 v213, v90, v91\n"
    "v_cvt_pk_bf16_f32 v214, v92, v93\n"
    "v_cvt_pk_bf16_f32 v215, v94, v95\n"
    "v_exp_f32 v64, v64\n"
    "v_exp_f32 v65, v65\n"
    "v_xor_b32 v112, v243, v96\n"
    "v_xor_b32 v113, v243, v97\n"
    "v_xor_b32 v114, v243, v98\n"
    "v_xor_b32 v115, v243, v99\n"
    "v_xor_b32 v116, v243, v100\n"
    "v_xor_b32 v117, v243, v101\n"
    "v_xor_b32 v118, v243, v102\n"
    "v_xor_b32 v119, v243, v103\n"
    "v_exp_f32 v66, v66\n"
    "v_exp_f32 v67, v67\n"
    "v_cvt_pk_bf16_f32 v216, v64, v65\n"
    "v_exp_f32 v68, v68\n"
    "v_exp_f32 v69, v69\n"
    "v_cvt_pk_bf16_f32 v217, v66, v67\n"
    "v_mfma_f32_32x32x16_bf16 v[80:95], v[152:155], v[204:207], 0\n"
    "v_xor_b32 v120, v243, v104\n"
    "v_xor_b32 v121, v243, v105\n"
    "v_xor_b32 v122, v243, v106\n"
    "v_xor_b32 v123, v243, v107\n"
    "v_xor_b32 v124, v243, v108\n"
    "v_xor_b32 v125, v243, v109\n"
    "v_xor_b32 v126, v243, v110\n"
    "v_xor_b32 v127, v243, v111\n"
    "v_exp_f32 v70, v70\n"
    "v_exp_f32 v71, v71\n"
    "v_cvt_pk_bf16_f32 v218, v68, v69\n"
    "v_exp_f32 v72, v72\n"
    "v_cvt_pk_bf16_f32 v219, v70, v71\n"
    "v_exp_f32 v73, v73\n"
    "v_exp_f32 v74, v74\n"
    "v_mfma_f32_32x32x16_bf16 v[0:15], v[168:171], v[216:219], 0\n"
    "v_exp_f32 v75, v75\n"
    "v_cvt_pk_bf16_f32 v220, v72, v73\n"
    "v_exp_f32 v76, v76\n"
    "v_exp_f32 v77, v77\n"
    "v_cvt_pk_bf16_f32 v221, v74, v75\n"
    "v_exp_f32 v78, v78\n"
    "v_exp_f32 v79, v79\n"
    "v_cvt_pk_bf16_f32 v222, v76, v77\n"
    "v_cvt_pk_bf16_f32 v223, v78, v79\n"
    "v_exp_f32 v80, v80\n"
    "v_exp_f32 v81, v81\n"
    "v_mfma_f32_32x32x16_bf16 v[0:15], v[172:175], v[220:223], v[0:15]\n"
    "s_barrier\n"
    "v_exp_f32 v82, v82\n"
    "v_exp_f32 v83, v83\n"
    "v_cvt_pk_bf16_f32 v216, v80, v81\n"
    "v_exp_f32 v84, v84\n"
    "v_exp_f32 v85, v85\n"
    "v_cvt_pk_bf16_f32 v217, v82, v83\n"
    "v_mfma_f32_32x32x16_bf16 v[64:79], v[156:159], v[204:207], v[96:111]\n"
    "v_exp_f32 v86, v86\n"
    "v_exp_f32 v87, v87\n"
    "v_cvt_pk_bf16_f32 v218, v84, v85\n"
    "v_exp_f32 v88, v88\n"
    "v_cvt_pk_bf16_f32 v219, v86, v87\n"
    "v_exp_f32 v89, v89\n"
    "v_exp_f32 v90, v90\n"
    "v_mfma_f32_32x32x16_bf16 v[16:31], v[168:171], v[216:219], 0\n"
    "v_exp_f32 v91, v91\n"
    "v_cvt_pk_bf16_f32 v220, v88, v89\n"
    "v_exp_f32 v92, v92\n"
    "v_exp_f32 v93, v93\n"
    "v_cvt_pk_bf16_f32 v221, v90, v91\n"
    "v_exp_f32 v94, v94\n"
    "v_exp_f32 v95, v95\n"
    "v_cvt_pk_bf16_f32 v222, v92, v93\n"
    "v_cvt_pk_bf16_f32 v223, v94, v95\n"
    "s_waitcnt vmcnt(12)\n"
    "v_cmp_eq_u64 vcc, s[64:65], v[248:249]\n"
    "s_nop 0\n"
    "s_and_b64 vcc, vcc, s[74:75]\n"
    "s_cmp_eq_u64 vcc, s[74:75]\n"
    "s_cbranch_scc0 L_slow_hpoll%=\n"
    "L_ok_hpoll%=:\n"
    "s_mov_b64 exec, s[70:71]\n"
    "buffer_load_dwordx4 a[96:99], v237, s[52:55], s79 offen offset:3072 sc1\n"
    "buffer_load_dwordx4 a[100:103], v238, s[52:55], s79 offen offset:3072 sc1\n"
    "s_mov_b64 exec, -1\n"
    "buffer_load_dwordx4 a[28:31], v237, s[52:55], s79 offen offset:1024 sc1\n"
    "s_mov_b64 exec, s[68:69]\n"
    "buffer_load_dwordx4 a[88:91], v238, s[52:55], s79 offen offset:2048 sc1\n"
    "buffer_load_dwordx4 a[92:95], v238, s[52:55], s79 offen offset:3072 sc1\n"
    "s_mov_b64 exec, -1\n"
    "buffer_load_dwordx4 a[24:27], v237, s[52:55], s79 offen offset:0 sc1\n"
    "s_mov_b64 exec, s[68:69]\n"
    "buffer_load_dwordx4 a[80:83], v238, s[52:55], s79 offen offset:0 sc1\n"
    "buffer_load_dwordx4 a[84:87], v238, s[52:55], s79 offen offset:1024 sc1\n"
    "s_mov_b64 exec, -1\n"
    "buffer_load_dwordx4 a[20:23], v237, s[52:55], s78 offen offset:1024 sc1\n"
    "s_mov_b64 exec, s[68:69]\n"
    "buffer_load_dwordx4 a[72:75], v238, s[52:55], s78 offen offset:2048 sc1\n"
    "buffer_load_dwordx4 a[76:79], v238, s[52:55], s78 offen offset:3072 sc1\n"
    "s_mov_b64 exec, -1\n"
    "v_exp_f32 v64, v64\n"
    "v_exp_f32 v65, v65\n"
    "v_mfma_f32_32x32x16_bf16 v[16:31], v[172:175], v[220:223], v[16:31]\n"
    "s_add_u32 m0, s21, 0\n"
    "s_lshl4_add_u32 s25, s18, s24\n"
    "buffer_load_dwordx4 v230, s[44:47], s25 offen lds\n"
    "v_exp_f32 v66, v66\n"
    "v_exp_f32 v67, v67\n"
    "v_cvt_pk_bf16_f32 v216, v64, v65\n"
    "v_exp_f32 v68, v68\n"
    "v_exp_f32 v69, v69\n"
    "v_cvt_pk_bf16_f32 v217, v66, v67\n"
    "v_mfma_f32_32x32x16_bf16 v[80:95], v[164:167], v[212:215], v[128:143]\n"
    "v_exp_f32 v70, v70\n"
    "v_exp_f32 v71, v71\n"
    "v_cvt_pk_bf16_f32 v218, v68, v69\n"
    "v_exp_f32 v72, v72\n"
    "v_cvt_pk_bf16_f32 v219, v70, v71\n"
    "v_exp_f32 v73, v73\n"
    "v_exp_f32 v74, v74\n"
    "v_mfma_f32_32x32x16_bf16 v[16:31], v[176:179], v[216:219], v[16:31]\n"
    "v_exp_f32 v75, v75\n"
    "v_cvt_pk_bf16_f32 v220, v72, v73\n"
    "v_exp_f32 v76, v76\n"
    "v_exp_f32 v77, v77\n"
    "v_cvt_pk_bf16_f32 v221, v74, v75\n"
    "v_exp_f32 v78, v78\n"
    "v_exp_f32 v79, v79\n"
    "v_cvt_pk_bf16_f32 v222, v76, v77\n"
    "v_cvt_pk_bf16_f32 v223, v78, v79\n"
    "v_exp_f32 v80, v80\n"
    "v_exp_f32 v81, v81\n"
    "v_mfma_f32_32x32x16_bf16 v[16:31], v[180:183], v[220:223], v[16:31]\n"
    "global_load_dwordx2 v[248:249], v251, s[26:27] sc1\n"
    "v_exp_f32 v82, v82\n"
    "v_exp_f32 v83, v83\n"
    "v_cvt_pk_bf16_f32 v216, v80, v81\n"
    "v_exp_f32 v84, v84\n"
    "v_exp_f32 v85, v85\n"
    "v_cvt_pk_bf16_f32 v217, v82, v83\n"
    "v_mfma_f32_32x32x16_bf16 v[64:79], v[160:163], v[212:215], v[136:151]\n"
    "buffer_load_dwordx4 v231, s[44:47], s25 offen offset:1024 lds\n"
    "v_exp_f32 v86, v86\n"
    "v_exp_f32 v87, v87\n"
    "v_cvt_pk_bf16_f32 v218, v84, v85\n"
    "v_exp_f32 v88, v88\n"
    "v_cvt_pk_bf16_f32 v219, v86, v87\n"
    "v_exp_f32 v89, v89\n"
    "v_exp_f32 v90, v90\n"
    "v_mfma_f32_32x32x16_bf16 v[48:63], v[192:195], v[216:219], 0\n"
    "v_exp_f32 v91, v91\n"
    "v_cvt_pk_bf16_f32 v220, v88, v89\n"
    "v_exp_f32 v92, v92\n"
    "v_exp_f32 v93, v93\n"
    "v_cvt_pk_bf16_f32 v221, v90, v91\n"
    "v_exp_f32 v94, v94\n"
    "v_exp_f32 v95, v95\n"
    "v_cvt_pk_bf16_f32 v222, v92, v93\n"
    "v_cvt_pk_bf16_f32 v223, v94, v95\n"
    "v_mfma_f32_32x32x16_bf16 v[80:95], v[160:163], v[208:211], v[128:143]\n"
    "v_exp_f32 v72, v72\n"
    "v_exp_f32 v73, v73\n"
    "v_exp_f32 v74, v74\n"
    "v_exp_f32 v75, v75\n"
    "v_mfma_f32_32x32x16_bf16 v[48:63], v[196:199], v[220:223], v[48:63]\n"
    "buffer_load_dwordx4 a[16:19], v237, s[52:55], s78 offen offset:0 sc1\n"
    "s_mov_b64 exec, s[68:69]\n"
    "buffer_load_dwordx4 a[64:67], v238, s[52:55], s78 offen offset:0 sc1\n"
    "buffer_load_dwordx4 a[68:71], v238, s[52:55], s78 offen offset:1024 sc1\n"
    "s_mov_b64 exec, -1\n"
    "v_cvt_pk_bf16_f32 v220, v72, v73\n"
    "v_exp_f32 v76, v76\n"
    "v_exp_f32 v77, v77\n"
    "v_cvt_pk_bf16_f32 v221, v74, v75\n"
    "v_exp_f32 v78, v78\n"
    "v_exp_f32 v79, v79\n"
    "v_cvt_pk_bf16_f32 v222, v76, v77\n"
    "v_cvt_pk_bf16_f32 v223, v78, v79\n"
    "buffer_load_dwordx4 v232, s[44:47], s25 offen offset:2048 lds\n"
    "v_exp_f32 v80, v80\n"
    "v_exp_f32 v81, v81\n"
    "v_mfma_f32_32x32x16_bf16 v[48:63], v[188:191], v[220:223], v[48:63]\n"
    "buffer_load_dwordx4 a[12:15], v237, s[52:55], s77 offen offset:1024 sc1\n"
    "s_mov_b64 exec, s[68:69]\n"
    "buffer_load_dwordx4 a[56:59], v238, s[52:55], s77 offen offset:2048 sc1\n"
    "buffer_load_dwordx4 a[60:63], v238, s[52:55], s77 offen offset:3072 sc1\n"
    "s_mov_b64 exec, -1\n"
    "v_exp_f32 v82, v82\n"
    "v_exp_f32 v83, v83\n"
    "v_cvt_pk_bf16_f32 v216, v80, v81\n"
    "v_exp_f32 v84, v84\n"
    "v_exp_f32 v85, v85\n"
    "v_cvt_pk_bf16_f32 v217, v82, v83\n"
    "s_waitcnt vmcnt(19)\n"
    "v_mfma_f32_32x32x16_bf16 v[64:79], a[96:99], v[208:211], v[136:151]\n"
    "buffer_load_dwordx4 v233, s[44:47], s25 offen offset:3072 lds\n"
    "v_exp_f32 v86, v86\n"
    "v_exp_f32 v87, v87\n"
    "v_cvt_pk_bf16_f32 v218, v84, v85\n"
    "v_exp_f32 v88, v88\n"
    "v_cvt_pk_bf16_f32 v219, v86, v87\n"
    "v_exp_f32 v89, v89\n"
    "v_exp_f32 v90, v90\n"
    "v_mfma_f32_32x32x16_bf16 v[32:47], v[184:187], v[216:219], 0\n"
    "v_exp_f32 v91, v91\n"
    "v_cvt_pk_bf16_f32 v220, v88, v89\n"
    "v_exp_f32 v92, v92\n"
    "v_exp_f32 v93, v93\n"
    "v_cvt_pk_bf16_f32 v221, v90, v91\n"
    "v_exp_f32 v94, v94\n"
    "v_exp_f32 v95, v95\n"
    "v_cvt_pk_bf16_f32 v222, v92, v93\n"
    "v_cvt_pk_bf16_f32 v223, v94, v95\n"
    "s_waitcnt vmcnt(19)\n"
    "v_mfma_f32_32x32x16_bf16 v[80:95], a[28:31], v[200:203], 0\n"
    "v_exp_f32 v72, v72\n"
    "v_exp_f32 v73, v73\n"
    "v_exp_f32 v74, v74\n"
    "v_exp_f32 v75, v75\n"
    "v_mfma_f32_32x32x16_bf16 v[32:47], v[188:191], v[220:223], v[32:47]\n"
    "buffer_load_dwordx4 a[8:11], v237, s[52:55], s77 offen offset:0 sc1\n"
    "s_mov_b64 exec, s[68:69]\n"
    "buffer_load_dwordx4 a[48:51], v238, s[52:55], s77 offen offset:0 sc1\n"
    "buffer_load_dwordx4 a[52:55], v238, s[52:55], s77 offen offset:1024 sc1\n"
    "s_mov_b64 exec, -1\n"
    "v_cvt_pk_bf16_f32 v220, v72, v73\n"
    "v_exp_f32 v76, v76\n"
    "v_exp_f32 v77, v77\n"
    "v_cvt_pk_bf16_f32 v221, v74, v75\n"
    "v_exp_f32 v78, v78\n"
    "v_exp_f32 v79, v79\n"
    "v_cvt_pk_bf16_f32 v222, v76, v77\n"
    "v_cvt_pk_bf16_f32 v223, v78, v79\n"
    "s_add_u32 m0, s21, 16384\n"
    "s_lshl4_add_u32 s25, s19, s24\n"
    "buffer_load_dwordx4 v230, s[44:47], s25 offen lds\n"
    "v_exp_f32 v80, v80\n"
    "v_exp_f32 v81, v81\n"
    "v_mfma_f32_32x32x16_bf16 v[32:47], a[100:103], v[220:223], v[32:47]\n"
    "buffer_load_dwordx4 a[4:7], v237, s[52:55], s76 offen offset:1024 sc1\n"
    "s_mov_b64 exec, s[68:69]\n"
    "buffer_load_dwordx4 a[40:43], v238, s[52:55], s76 offen offset:2048 sc1\n"
    "buffer_load_dwordx4 a[44:47], v238, s[52:55], s76 offen offset:3072 sc1\n"
    "s_mov_b64 exec, -1\n"
    "v_exp_f32 v82, v82\n"
    "v_exp_f32 v83, v83\n"
    "v_cvt_pk_bf16_f32 v216, v80, v81\n"
    "v_exp_f32 v84, v84\n"
    "v_exp_f32 v85, v85\n"
    "v_cvt_pk_bf16_f32 v217, v82, v83\n"
    "s_waitcnt vmcnt(23)\n"
    "v_mfma_f32_32x32x16_bf16 v[64:79], a[24:27], v[200:203], 0\n"
    "buffer_load_dwordx4 v231, s[44:47], s25 offen offset:1024 lds\n"
    "v_exp_f32 v86, v86\n"
    "v_exp_f32 v87, v87\n"
    "v_cvt_pk_bf16_f32 v218, v84, v85\n"
    "v_exp_f32 v88, v88\n"
    "v_cvt_pk_bf16_f32 v219, v86, v87\n"
    "v_exp_f32 v89, v89\n"
    "v_exp_f32 v90, v90\n"
    "v_mfma_f32_32x32x16_bf16 v[0:15], a[88:91], v[216:219], v[0:15]\n"
    "v_exp_f32 v91, v91\n"
    "v_cvt_pk_bf16_f32 v220, v88, v89\n"
    "v_exp_f32 v92, v92\n"
    "v_exp_f32 v93, v93\n"
    "v_cvt_pk_bf16_f32 v221, v90, v91\n"
    "v_exp_f32 v94, v94\n"
    "v_exp_f32 v95, v95\n"
    "v_cvt_pk_bf16_f32 v222, v92, v93\n"
    "v_cvt_pk_bf16_f32 v223, v94, v95\n"
    "v_pk_add_f32 v[224:225], v[56:57], v[58:59]\n"
    "v_pk_add_f32 v[226:227], v[60:61], v[62:63]\n"
    "v_subrev_u32 v228, 0x12800000, v48\n"
    "v_pk_add_f32 v[224:225], v[224:225], v[226:227]\n"
    "v_cmp_gt_u32 vcc, 0x69000000, v228\n"
    "v_add_f32 v224, v224, v225\n"
    "v_cmp_class_f32_e64 s[96:97], v224, s93\n"
    "v_rcp_f32 v224, v48\n"
    "s_and_b64 vcc, vcc, s[96:97]\n"
    "s_cmp_eq_u64 vcc, -1\n"
    "s_cbranch_scc0 L_fb01%=\n"
    "L_norm01%=:\n"
    "v_mul_f32 v224, v224, v253\n"
    "v_pk_mul_f32 v[56:57], v[56:57], v[224:225] op_sel_hi:[1,0]\n"
    "v_pk_mul_f32 v[58:59], v[58:59], v[224:225] op_sel_hi:[1,0]\n"
    "v_pk_mul_f32 v[60:61], v[60:61], v[224:225] op_sel_hi:[1,0]\n"
    "v_pk_mul_f32 v[62:63], v[62:63], v[224:225] op_sel_hi:[1,0]\n"
    "v_cvt_pk_bf16_f32 v224, v56, v57\n"
    "v_cvt_pk_bf16_f32 v225, v58, v59\n"
    "v_cvt_pk_bf16_f32 v226, v60, v61\n"
    "v_cvt_pk_bf16_f32 v227, v62, v63\n"
    "v_min_u32 v224, -2, v224\n"
    "v_min_u32 v225, -2, v225\n"
    "v_min_u32 v226, -2, v226\n"
    "v_min_u32 v227, -2, v227\n"
    "buffer_store_dwordx4 v[224:227], v237, s[12:15], s17 offen sc1\n"
    "s_nop 1\n"
    "v_pk_add_f32 v[224:225], v[40:41], v[42:43]\n"
    "v_pk_add_f32 v[226:227], v[44:45], v[46:47]\n"
    "v_subrev_u32 v228, 0x12800000, v32\n"
    "v_pk_add_f32 v[224:225], v[224:225], v[226:227]\n"
    "v_cmp_gt_u32 vcc, 0x69000000, v228\n"
    "v_add_f32 v224, v224, v225\n"
    "v_cmp_class_f32_e64 s[96:97], v224, s93\n"
    "v_rcp_f32 v224, v32\n"
    "s_and_b64 vcc, vcc, s[96:97]\n"
    "s_cmp_eq_u64 vcc, -1\n"
    "s_cbranch_scc0 L_fb00%=\n"
    "L_norm00%=:\n"
    "v_mul_f32 v224, v224, v252\n"
    "v_pk_mul_f32 v[40:41], v[40:41], v[224:225] op_sel_hi:[1,0]\n"
    "v_pk_mul_f32 v[42:43], v[42:43], v[224:225] op_sel_hi:[1,0]\n"
    "v_pk_mul_f32 v[44:45], v[44:45], v[224:225] op_sel_hi:[1,0]\n"
    "v_pk_mul_f32 v[46:47], v[46:47], v[224:225] op_sel_hi:[1,0]\n"
    "v_cvt_pk_bf16_f32 v224, v40, v41\n"
    "v_cvt_pk_bf16_f32 v225, v42, v43\n"
    "v_cvt_pk_bf16_f32 v226, v44, v45\n"
    "v_cvt_pk_bf16_f32 v227, v46, v47\n"
    "v_min_u32 v224, -2, v224\n"
    "v_min_u32 v225, -2, v225\n"
    "v_min_u32 v226, -2, v226\n"
    "v_min_u32 v227, -2, v227\n"
    "buffer_store_dwordx4 v[224:227], v237, s[12:15], 0 offen sc1\n"
    "v_exp_f32 v64, v64\n"
    "v_exp_f32 v65, v65\n"
    "v_mfma_f32_32x32x16_bf16 v[0:15], a[92:95], v[220:223], v[0:15]\n"
    "buffer_load_dwordx4 a[0:3], v237, s[52:55], s76 offen offset:0 sc1\n"
    "s_mov_b64 exec, s[68:69]\n"
    "buffer_load_dwordx4 a[32:35], v238, s[52:55], s76 offen offset:0 sc1\n"
    "buffer_load_dwordx4 a[36:39], v238, s[52:55], s76 offen offset:1024 sc1\n"
    "s_mov_b64 exec, -1\n"
    "v_exp_f32 v66, v66\n"
    "v_exp_f32 v67, v67\n"
    "v_cvt_pk_bf16_f32 v216, v64, v65\n"
    "v_exp_f32 v68, v68\n"
    "v_exp_f32 v69, v69\n"
    "v_cvt_pk_bf16_f32 v217, v66, v67\n"
    "s_waitcnt vmcnt(26)\n"
    "v_mfma_f32_32x32x16_bf16 v[80:95], a[20:23], v[200:203], 0\n"
    "buffer_load_dwordx4 v232, s[44:47], s25 offen offset:2048 lds\n"
    "v_exp_f32 v70, v70\n"
    "v_exp_f32 v71, v71\n"
    "v_cvt_pk_bf16_f32 v218, v68, v69\n"
    "v_exp_f32 v72, v72\n"
    "v_cvt_pk_bf16_f32 v219, v70, v71\n"
    "v_exp_f32 v73, v73\n"
    "v_exp_f32 v74, v74\n"
    "v_mfma_f32_32x32x16_bf16 v[0:15], a[80:83], v[216:219], v[0:15]\n"
    "v_exp_f32 v75, v75\n"
    "v_cvt_pk_bf16_f32 v220, v72, v73\n"
    "v_exp_f32 v76, v76\n"
    "v_exp_f32 v77, v77\n"
    "v_cvt_pk_bf16_f32 v221, v74, v75\n"
    "v_exp_f32 v78, v78\n"
    "v_exp_f32 v79, v79\n"
    "v_cvt_pk_bf16_f32 v222, v76, v77\n"
    "v_cvt_pk_bf16_f32 v223, v78, v79\n"
    "v_exp_f32 v80, v80\n"
    "v_exp_f32 v81, v81\n"
    "v_mfma_f32_32x32x16_bf16 v[0:15], a[84:87], v[220:223], v[0:15]\n"
    "buffer_load_dwordx4 v233, s[44:47], s25 offen offset:3072 lds\n"
    "v_exp_f32 v82, v82\n"
    "v_exp_f32 v83, v83\n"
    "v_cvt_pk_bf16_f32 v216, v80, v81\n"
    "v_exp_f32 v84, v84\n"
    "v_exp_f32 v85, v85\n"
    "v_cvt_pk_bf16_f32 v217, v82, v83\n"
    "s_waitcnt vmcnt(22)\n"
    "v_mfma_f32_32x32x16_bf16 v[64:79], a[16:19], v[200:203], 0\n"
    "v_exp_f32 v86, v86\n"
    "v_exp_f32 v87, v87\n"
    "v_cvt_pk_bf16_f32 v218, v84, v85\n"
    "v_exp_f32 v88, v88\n"
    "v_cvt_pk_bf16_f32 v219, v86, v87\n"
    "v_exp_f32 v89, v89\n"
    "v_exp_f32 v90, v90\n"
    "v_mfma_f32_32x32x16_bf16 v[0:15], a[72:75], v[216:219], v[0:15]\n"
    "v_exp_f32 v91, v91\n"
    "v_cvt_pk_bf16_f32 v220, v88, v89\n"
    "v_exp_f32 v92, v92\n"
    "v_exp_f32 v93, v93\n"
    "v_cvt_pk_bf16_f32 v221, v90, v91\n"
    "v_exp_f32 v94, v94\n"
    "v_exp_f32 v95, v95\n"
    "v_cvt_pk_bf16_f32 v222, v92, v93\n"
    "v_cvt_pk_bf16_f32 v223, v94, v95\n"
    "v_cmp_eq_u64 vcc, s[66:67], v[248:249]\n"
    "s_nop 0\n"
    "s_cmp_eq_u64 vcc, -1\n"
    "s_cbranch_scc0 L_slow_cpoll%=\n"
    "L_ok_cpoll%=:\n"
    "v_exp_f32 v64, v64\n"
    "v_exp_f32 v65, v65\n"
    "v_mfma_f32_32x32x16_bf16 v[0:15], a[76:79], v[220:223], v[0:15]\n"
    "v_exp_f32 v66, v66\n"
    "v_exp_f32 v67, v67\n"
    "v_cvt_pk_bf16_f32 v216, v64, v65\n"
    "v_exp_f32 v68, v68\n"
    "v_exp_f32 v69, v69\n"
    "v_cvt_pk_bf16_f32 v217, v66, v67\n"
    "s_waitcnt vmcnt(18)\n"
    "v_mfma_f32_32x32x16_bf16 v[80:95], a[12:15], v[200:203], 0\n"
    "v_exp_f32 v70, v70\n"
    "v_exp_f32 v71, v71\n"
    "v_cvt_pk_bf16_f32 v218, v68, v69\n"
    "v_exp_f32 v72, v72\n"
    "v_cvt_pk_bf16_f32 v219, v70, v71\n"
    "v_exp_f32 v73, v73\n"
    "v_exp_f32 v74, v74\n"
    "v_mfma_f32_32x32x16_bf16 v[0:15], a[64:67], v[216:219], v[0:15]\n"
    "v_exp_f32 v75, v75\n"
    "v_cvt_pk_bf16_f32 v220, v72, v73\n"
    "v_exp_f32 v76, v76\n"
    "v_exp_f32 v77, v77\n"
    "v_cvt_pk_bf16_f32 v221, v74, v75\n"
    "v_exp_f32 v78, v78\n"
    "v_exp_f32 v79, v79\n"
    "v_cvt_pk_bf16_f32 v222, v76, v77\n"
    "v_cvt_pk_bf16_f32 v223, v78, v79\n"
    "v_exp_f32 v80, v80\n"
    "v_exp_f32 v81, v81\n"
    "v_mfma_f32_32x32x16_bf16 v[0:15], a[68:71], v[220:223], v[0:15]\n"
    "v_exp_f32 v82, v82\n"
    "v_exp_f32 v83, v83\n"
    "v_cvt_pk_bf16_f32 v216, v80, v81\n"
    "v_exp_f32 v84, v84\n"
    "v_exp_f32 v85, v85\n"
    "v_cvt_pk_bf16_f32 v217, v82, v83\n"
    "s_waitcnt vmcnt(14)\n"
    "v_mfma_f32_32x32x16_bf16 v[64:79], a[8:11], v[200:203], 0\n"
    "v_exp_f32 v86, v86\n"
    "v_exp_f32 v87, v87\n"
    "v_cvt_pk_bf16_f32 v218, v84, v85\n"
    "v_exp_f32 v88, v88\n"
    "v_cvt_pk_bf16_f32 v219, v86, v87\n"
    "v_exp_f32 v89, v89\n"
    "v_exp_f32 v90, v90\n"
    "v_mfma_f32_32x32x16_bf16 v[0:15], a[56:59], v[216:219], v[0:15]\n"
    "v_exp_f32 v91, v91\n"
    "v_cvt_pk_bf16_f32 v220, v88, v89\n"
    "v_exp_f32 v92, v92\n"
    "v_exp_f32 v93, v93\n"
    "v_cvt_pk_bf16_f32 v221, v90, v91\n"
    "v_exp_f32 v94, v94\n"
    "v_exp_f32 v95, v95\n"
    "v_cvt_pk_bf16_f32 v222, v92, v93\n"
    "v_cvt_pk_bf16_f32 v223, v94, v95\n"
    "v_exp_f32 v64, v64\n"
    "v_exp_f32 v65, v65\n"
    "v_mfma_f32_32x32x16_bf16 v[0:15], a[60:63], v[220:223], v[0:15]\n"
    "s_add_u32 s98, s18, 0\n"
    "buffer_load_dwordx4 v[128:131], v237, s[88:91], s98 offen sc1\n"
    "buffer_load_dwordx4 v[132:135], v237, s[88:91], s98 offen offset:1024 sc1\n"
    "v_exp_f32 v66, v66\n"
    "v_exp_f32 v67, v67\n"
    "v_cvt_pk_bf16_f32 v216, v64, v65\n"
    "v_exp_f32 v68, v68\n"
    "v_exp_f32 v69, v69\n"
    "v_cvt_pk_bf16_f32 v217, v66, v67\n"
    "s_waitcnt vmcnt(12)\n"
    "v_mfma_f32_32x32x16_bf16 v[80:95], a[4:7], v[200:203], 0\n"
    "v_exp_f32 v70, v70\n"
    "v_exp_f32 v71, v71\n"
    "v_cvt_pk_bf16_f32 v218, v68, v69\n"
    "v_exp_f32 v72, v72\n"
    "v_cvt_pk_bf16_f32 v219, v70, v71\n"
    "v_exp_f32 v73, v73\n"
    "v_exp_f32 v74, v74\n"
    "v_mfma_f32_32x32x16_bf16 v[0:15], a[48:51], v[216:219], v[0:15]\n"
    "v_exp_f32 v75, v75\n"
    "v_cvt_pk_bf16_f32 v220, v72, v73\n"
    "v_exp_f32 v76, v76\n"
    "v_exp_f32 v77, v77\n"
    "v_cvt_pk_bf16_f32 v221, v74, v75\n"
    "v_exp_f32 v78, v78\n"
    "v_exp_f32 v79, v79\n"
    "v_cvt_pk_bf16_f32 v222, v76, v77\n"
    "v_cvt_pk_bf16_f32 v223, v78, v79\n"
    "v_exp_f32 v80, v80\n"
    "v_exp_f32 v81, v81\n"
    "v_mfma_f32_32x32x16_bf16 v[0:15], a[52:55], v[220:223], v[0:15]\n"
    "s_add_u32 s98, s18, 65536\n"
    "buffer_load_dwordx4 v[160:163], v237, s[88:91], s98 offen sc1\n"
    "buffer_load_dwordx4 v[164:167], v237, s[88:91], s98 offen offset:1024 sc1\n"
    "v_exp_f32 v82, v82\n"
    "v_exp_f32 v83, v83\n"
    "v_cvt_pk_bf16_f32 v216, v80, v81\n"
    "v_exp_f32 v84, v84\n"
    "v_exp_f32 v85, v85\n"
    "v_cvt_pk_bf16_f32 v217, v82, v83\n"
    "s_waitcnt vmcnt(8)\n"
    "v_mfma_f32_32x32x16_bf16 v[64:79], a[0:3], v[200:203], v[112:127]\n"
    "v_exp_f32 v86, v86\n"
    "v_exp_f32 v87, v87\n"
    "v_cvt_pk_bf16_f32 v218, v84, v85\n"
    "v_exp_f32 v88, v88\n"
    "v_cvt_pk_bf16_f32 v219, v86, v87\n"
    "v_exp_f32 v89, v89\n"
    "v_exp_f32 v90, v90\n"
    "v_mfma_f32_32x32x16_bf16 v[0:15], a[40:43], v[216:219], v[0:15]\n"
    "v_exp_f32 v91, v91\n"
    "v_cvt_pk_bf16_f32 v220, v88, v89\n"
    "v_exp_f32 v92, v92\n"
    "v_exp_f32 v93, v93\n"
    "v_cvt_pk_bf16_f32 v221, v90, v91\n"
    "v_exp_f32 v94, v94\n"
    "v_exp_f32 v95, v95\n"
    "v_cvt_pk_bf16_f32 v222, v92, v93\n"
    "v_cvt_pk_bf16_f32 v223, v94, v95\n"
    "v_exp_f32 v64, v64\n"
    "v_exp_f32 v65, v65\n"
    "v_mfma_f32_32x32x16_bf16 v[0:15], a[44:47], v[220:223], v[0:15]\n"
    "s_add_u32 s98, s19, 0\n"
    "buffer_load_dwordx4 v[136:139], v237, s[88:91], s98 offen sc1\n"
    "buffer_load_dwordx4 v[140:143], v237, s[88:91], s98 offen offset:1024 sc1\n"
    "v_exp_f32 v66, v66\n"
    "v_exp_f32 v67, v67\n"
    "v_cvt_pk_bf16_f32 v216, v64, v65\n"
    "v_exp_f32 v68, v68\n"
    "v_exp_f32 v69, v69\n"
    "v_cvt_pk_bf16_f32 v217, v66, v67\n"
    "v_mfma_f32_32x32x16_bf16 v[80:95], a[28:31], v[204:207], 0\n"
    "v_exp_f32 v70, v70\n"
    "v_exp_f32 v71, v71\n"
    "v_cvt_pk_bf16_f32 v218, v68, v69\n"
    "v_exp_f32 v72, v72\n"
    "v_cvt_pk_bf16_f32 v219, v70, v71\n"
    "v_exp_f32 v73, v73\n"
    "v_exp_f32 v74, v74\n"
    "s_waitcnt vmcnt(8)\n"
    "v_mfma_f32_32x32x16_bf16 v[0:15], a[32:35], v[216:219], v[0:15]\n"
    "v_exp_f32 v75, v75\n"
    "v_cvt_pk_bf16_f32 v220, v72, v73\n"
    "v_exp_f32 v76, v76\n"
    "v_exp_f32 v77, v77\n"
    "v_cvt_pk_bf16_f32 v221, v74, v75\n"
    "v_exp_f32 v78, v78\n"
    "v_exp_f32 v79, v79\n"
    "v_cvt_pk_bf16_f32 v222, v76, v77\n"
    "v_cvt_pk_bf16_f32 v223, v78, v79\n"
    "v_exp_f32 v80, v80\n"
    "v_exp_f32 v81, v81\n"
    "v_mfma_f32_32x32x16_bf16 v[0:15], a[36:39], v[220:223], v[0:15]\n"
    "s_add_u32 s98, s19, 65536\n"
    "buffer_load_dwordx4 v[168:171], v237, s[88:91], s98 offen sc1\n"
    "buffer_load_dwordx4 v[172:175], v237, s[88:91], s98 offen offset:1024 sc1\n"
    "v_exp_f32 v82, v82\n"
    "v_exp_f32 v83, v83\n"
    "v_cvt_pk_bf16_f32 v216, v80, v81\n"
    "v_exp_f32 v84, v84\n"
    "v_exp_f32 v85, v85\n"
    "v_cvt_pk_bf16_f32 v217, v82, v83\n"
    "v_mfma_f32_32x32x16_bf16 v[64:79], a[24:27], v[204:207], 0\n"
    "v_exp_f32 v86, v86\n"
    "v_exp_f32 v87, v87\n"
    "v_cvt_pk_bf16_f32 v218, v84, v85\n"
    "v_exp_f32 v88, v88\n"
    "v_cvt_pk_bf16_f32 v219, v86, v87\n"
    "v_exp_f32 v89, v89\n"
    "v_exp_f32 v90, v90\n"
    "v_mfma_f32_32x32x16_bf16 v[16:31], a[88:91], v[216:219], v[16:31]\n"
    "v_exp_f32 v91, v91\n"
    "v_cvt_pk_bf16_f32 v220, v88, v89\n"
    "v_exp_f32 v92, v92\n"
    "v_exp_f32 v93, v93\n"
    "v_cvt_pk_bf16_f32 v221, v90, v91\n"
    "v_exp_f32 v94, v94\n"
    "v_exp_f32 v95, v95\n"
    "v_cvt_pk_bf16_f32 v222, v92, v93\n"
    "v_cvt_pk_bf16_f32 v223, v94, v95\n"
    "v_pk_add_f32 v[224:225], v[0:1], v[2:3]\n"
    "v_pk_add_f32 v[226:227], v[4:5], v[6:7]\n"
    "v_subrev_u32 v228, 0x12800000, v8\n"
    "v_pk_add_f32 v[224:225], v[224:225], v[226:227]\n"
    "v_cmp_gt_u32 vcc, 0x69000000, v228\n"
    "v_add_f32 v224, v224, v225\n"
    "v_cmp_class_f32_e64 s[96:97], v224, s93\n"
    "v_rcp_f32 v224, v8\n"
    "s_and_b64 vcc, vcc, s[96:97]\n"
    "s_cmp_eq_u64 vcc, -1\n"
    "s_cbranch_scc0 L_fb10%=\n"
    "L_norm10%=:\n"
    "v_fma_f32 v224, -v224, v252, v224\n"
    "v_pk_mul_f32 v[0:1], v[0:1], v[224:225] op_sel_hi:[1,0]\n"
    "v_pk_mul_f32 v[2:3], v[2:3], v[224:225] op_sel_hi:[1,0]\n"
    "v_pk_mul_f32 v[4:5], v[4:5], v[224:225] op_sel_hi:[1,0]\n"
    "v_pk_mul_f32 v[6:7], v[6:7], v[224:225] op_sel_hi:[1,0]\n"
    "v_cvt_pk_bf16_f32 v224, v0, v1\n"
    "v_cvt_pk_bf16_f32 v225, v2, v3\n"
    "v_cvt_pk_bf16_f32 v226, v4, v5\n"
    "v_cvt_pk_bf16_f32 v227, v6, v7\n"
    "v_min_u32 v224, -2, v224\n"
    "v_min_u32 v225, -2, v225\n"
    "v_min_u32 v226, -2, v226\n"
    "v_min_u32 v227, -2, v227\n"
    "buffer_store_dwordx4 v[224:227], v237, s[12:15], s16 offen sc1\n"
    "v_exp_f32 v64, v64\n"
    "v_exp_f32 v65, v65\n"
    "v_mfma_f32_32x32x16_bf16 v[16:31], a[92:95], v[220:223], v[16:31]\n"
    "s_add_u32 s98, s22, 0\n"
    "buffer_load_dwordx4 v[144:147], v237, s[88:91], s98 offen sc1\n"
    "buffer_load_dwordx4 v[148:151], v237, s[88:91], s98 offen offset:1024 sc1\n"
    "v_exp_f32 v66, v66\n"
    "v_exp_f32 v67, v67\n"
    "v_cvt_pk_bf16_f32 v216, v64, v65\n"
    "v_exp_f32 v68, v68\n"
    "v_exp_f32 v69, v69\n"
    "v_cvt_pk_bf16_f32 v217, v66, v67\n"
    "v_mfma_f32_32x32x16_bf16 v[80:95], a[20:23], v[204:207], 0\n"
    "v_exp_f32 v70, v70\n"
    "v_exp_f32 v71, v71\n"
    "v_cvt_pk_bf16_f32 v218, v68, v69\n"
    "v_exp_f32 v72, v72\n"
    "v_cvt_pk_bf16_f32 v219, v70, v71\n"
    "v_exp_f32 v73, v73\n"
    "v_exp_f32 v74, v74\n"
    "v_mfma_f32_32x32x16_bf16 v[16:31], a[80:83], v[216:219], v[16:31]\n"
    "v_exp_f32 v75, v75\n"
    "v_cvt_pk_bf16_f32 v220, v72, v73\n"
    "v_exp_f32 v76, v76\n"
    "v_exp_f32 v77, v77\n"
    "v_cvt_pk_bf16_f32 v221, v74, v75\n"
    "v_exp_f32 v78, v78\n"
    "v_exp_f32 v79, v79\n"
    "v_cvt_pk_bf16_f32 v222, v76, v77\n"
    "v_cvt_pk_bf16_f32 v223, v78, v79\n"
    "v_exp_f32 v80, v80\n"
    "v_exp_f32 v81, v81\n"
    "v_mfma_f32_32x32x16_bf16 v[16:31], a[84:87], v[220:223], v[16:31]\n"
    "s_add_u32 s98, s22, 65536\n"
    "buffer_load_dwordx4 v[176:179], v237, s[88:91], s98 offen sc1\n"
    "buffer_load_dwordx4 v[180:183], v237, s[88:91], s98 offen offset:1024 sc1\n"
    "v_exp_f32 v82, v82\n"
    "v_exp_f32 v83, v83\n"
    "v_cvt_pk_bf16_f32 v216, v80, v81\n"
    "v_exp_f32 v84, v84\n"
    "v_exp_f32 v85, v85\n"
    "v_cvt_pk_bf16_f32 v217, v82, v83\n"
    "v_mfma_f32_32x32x16_bf16 v[64:79], a[16:19], v[204:207], 0\n"
    "v_exp_f32 v86, v86\n"
    "v_exp_f32 v87, v87\n"
    "v_cvt_pk_bf16_f32 v218, v84, v85\n"
    "v_exp_f32 v88, v88\n"
    "v_cvt_pk_bf16_f32 v219, v86, v87\n"
    "v_exp_f32 v89, v89\n"
    "v_exp_f32 v90, v90\n"
    "v_mfma_f32_32x32x16_bf16 v[16:31], a[72:75], v[216:219], v[16:31]\n"
    "v_exp_f32 v91, v91\n"
    "v_cvt_pk_bf16_f32 v220, v88, v89\n"
    "v_exp_f32 v92, v92\n"
    "v_exp_f32 v93, v93\n"
    "v_cvt_pk_bf16_f32 v221, v90, v91\n"
    "v_exp_f32 v94, v94\n"
    "v_exp_f32 v95, v95\n"
    "v_cvt_pk_bf16_f32 v222, v92, v93\n"
    "v_cvt_pk_bf16_f32 v223, v94, v95\n"
    "v_exp_f32 v64, v64\n"
    "v_exp_f32 v65, v65\n"
    "v_mfma_f32_32x32x16_bf16 v[16:31], a[76:79], v[220:223], v[16:31]\n"
    "s_add_u32 s98, s23, 0\n"
    "buffer_load_dwordx4 v[152:155], v237, s[88:91], s98 offen sc1\n"
    "buffer_load_dwordx4 v[156:159], v237, s[88:91], s98 offen offset:1024 sc1\n"
    "v_exp_f32 v66, v66\n"
    "v_exp_f32 v67, v67\n"
    "v_cvt_pk_bf16_f32 v216, v64, v65\n"
    "v_exp_f32 v68, v68\n"
    "v_exp_f32 v69, v69\n"
    "v_cvt_pk_bf16_f32 v217, v66, v67\n"
    "v_mfma_f32_32x32x16_bf16 v[80:95], a[12:15], v[204:207], 0\n"
    "v_exp_f32 v70, v70\n"
    "v_exp_f32 v71, v71\n"
    "v_cvt_pk_bf16_f32 v218, v68, v69\n"
    "v_exp_f32 v72, v72\n"
    "v_cvt_pk_bf16_f32 v219, v70, v71\n"
    "v_exp_f32 v73, v73\n"
    "v_exp_f32 v74, v74\n"
    "v_mfma_f32_32x32x16_bf16 v[16:31], a[64:67], v[216:219], v[16:31]\n"
    "v_exp_f32 v75, v75\n"
    "v_cvt_pk_bf16_f32 v220, v72, v73\n"
    "v_exp_f32 v76, v76\n"
    "v_exp_f32 v77, v77\n"
    "v_cvt_pk_bf16_f32 v221, v74, v75\n"
    "v_exp_f32 v78, v78\n"
    "v_exp_f32 v79, v79\n"
    "v_cvt_pk_bf16_f32 v222, v76, v77\n"
    "v_cvt_pk_bf16_f32 v223, v78, v79\n"
    "v_exp_f32 v80, v80\n"
    "v_exp_f32 v81, v81\n"
    "v_mfma_f32_32x32x16_bf16 v[16:31], a[68:71], v[220:223], v[16:31]\n"
    "s_add_u32 s98, s23, 65536\n"
    "buffer_load_dwordx4 v[184:187], v237, s[88:91], s98 offen sc1\n"
    "buffer_load_dwordx4 v[188:191], v237, s[88:91], s98 offen offset:1024 sc1\n"
    "v_exp_f32 v82, v82\n"
    "v_exp_f32 v83, v83\n"
    "v_cvt_pk_bf16_f32 v216, v80, v81\n"
    "v_exp_f32 v84, v84\n"
    "v_exp_f32 v85, v85\n"
    "v_cvt_pk_bf16_f32 v217, v82, v83\n"
    "v_mfma_f32_32x32x16_bf16 v[64:79], a[8:11], v[204:207], 0\n"
    "v_exp_f32 v86, v86\n"
    "v_exp_f32 v87, v87\n"
    "v_cvt_pk_bf16_f32 v218, v84, v85\n"
    "v_exp_f32 v88, v88\n"
    "v_cvt_pk_bf16_f32 v219, v86, v87\n"
    "v_exp_f32 v89, v89\n"
    "v_exp_f32 v90, v90\n"
    "v_mfma_f32_32x32x16_bf16 v[16:31], a[56:59], v[216:219], v[16:31]\n"
    "v_exp_f32 v91, v91\n"
    "v_cvt_pk_bf16_f32 v220, v88, v89\n"
    "v_exp_f32 v92, v92\n"
    "v_exp_f32 v93, v93\n"
    "v_cvt_pk_bf16_f32 v221, v90, v91\n"
    "v_exp_f32 v94, v94\n"
    "v_exp_f32 v95, v95\n"
    "v_cvt_pk_bf16_f32 v222, v92, v93\n"
    "v_cvt_pk_bf16_f32 v223, v94, v95\n"
    "v_exp_f32 v64, v64\n"
    "v_exp_f32 v65, v65\n"
    "v_mfma_f32_32x32x16_bf16 v[16:31], a[60:63], v[220:223], v[16:31]\n"
    "v_exp_f32 v66, v66\n"
    "v_exp_f32 v67, v67\n"
    "v_cvt_pk_bf16_f32 v216, v64, v65\n"
    "v_exp_f32 v68, v68\n"
    "v_exp_f32 v69, v69\n"
    "v_cvt_pk_bf16_f32 v217, v66, v67\n"
    "v_mfma_f32_32x32x16_bf16 v[80:95], a[4:7], v[204:207], v[112:127]\n"
    "v_exp_f32 v70, v70\n"
    "v_exp_f32 v71, v71\n"
    "v_cvt_pk_bf16_f32 v218, v68, v69\n"
    "v_exp_f32 v72, v72\n"
    "v_cvt_pk_bf16_f32 v219, v70, v71\n"
    "v_exp_f32 v73, v73\n"
    "v_exp_f32 v74, v74\n"
    "v_mfma_f32_32x32x16_bf16 v[16:31], a[48:51], v[216:219], v[16:31]\n"
    "v_exp_f32 v75, v75\n"
    "v_cvt_pk_bf16_f32 v220, v72, v73\n"
    "v_exp_f32 v76, v76\n"
    "v_exp_f32 v77, v77\n"
    "v_cvt_pk_bf16_f32 v221, v74, v75\n"
    "v_exp_f32 v78, v78\n"
    "v_exp_f32 v79, v79\n"
    "v_cvt_pk_bf16_f32 v222, v76, v77\n"
    "v_cvt_pk_bf16_f32 v223, v78, v79\n"
    "v_exp_f32 v80, v80\n"
    "v_exp_f32 v81, v81\n"
    "v_mfma_f32_32x32x16_bf16 v[16:31], a[52:55], v[220:223], v[16:31]\n"
    "v_exp_f32 v82, v82\n"
    "v_exp_f32 v83, v83\n"
    "v_cvt_pk_bf16_f32 v216, v80, v81\n"
    "v_exp_f32 v84, v84\n"
    "v_exp_f32 v85, v85\n"
    "v_cvt_pk_bf16_f32 v217, v82, v83\n"
    "v_exp_f32 v86, v86\n"
    "v_exp_f32 v87, v87\n"
    "v_cvt_pk_bf16_f32 v218, v84, v85\n"
    "v_exp_f32 v88, v88\n"
    "v_cvt_pk_bf16_f32 v219, v86, v87\n"
    "v_exp_f32 v89, v89\n"
    "v_exp_f32 v90, v90\n"
    "v_mfma_f32_32x32x16_bf16 v[16:31], a[40:43], v[216:219], v[16:31]\n"
    "v_exp_f32 v91, v91\n"
    "v_cvt_pk_bf16_f32 v220, v88, v89\n"
    "v_exp_f32 v92, v92\n"
    "v_exp_f32 v93, v93\n"
    "v_cvt_pk_bf16_f32 v221, v90, v91\n"
    "v_exp_f32 v94, v94\n"
    "v_exp_f32 v95, v95\n"
    "v_cvt_pk_bf16_f32 v222, v92, v93\n"
    "v_cvt_pk_bf16_f32 v223, v94, v95\n"
    "s_nop 1\n"
    "v_mfma_f32_32x32x16_bf16 v[16:31], a[44:47], v[220:223], v[16:31]\n"
    "s_waitcnt vmcnt(17)\n"
    "ds_read_b64_tr_b16 a[104:105], v241 offset:0\n"
    "ds_read_b64_tr_b16 a[106:107], v241 offset:1024\n"
    "ds_read_b64_tr_b16 a[108:109], v242 offset:0\n"
    "ds_read_b64_tr_b16 a[110:111], v242 offset:1024\n"
    "ds_read_b64_tr_b16 a[112:113], v241 offset:2048\n"
    "ds_read_b64_tr_b16 a[114:115], v241 offset:3072\n"
    "ds_read_b64_tr_b16 a[116:117], v242 offset:2048\n"
    "ds_read_b64_tr_b16 a[118:119], v242 offset:3072\n"
    "s_nop 2\n"
    "v_pk_add_f32 v[224:225], v[16:17], v[18:19]\n"
    "v_pk_add_f32 v[226:227], v[20:21], v[22:23]\n"
    "v_subrev_u32 v228, 0x12800000, v24\n"
    "v_pk_add_f32 v[224:225], v[224:225], v[226:227]\n"
    "v_cmp_gt_u32 vcc, 0x69000000, v228\n"
    "v_add_f32 v224, v224, v225\n"
    "v_cmp_class_f32_e64 s[96:97], v224, s93\n"
    "v_rcp_f32 v224, v24\n"
    "s_and_b64 vcc, vcc, s[96:97]\n"
    "s_cmp_eq_u64 vcc, -1\n"
    "s_cbranch_scc0 L_fb11%=\n"
    "L_norm11%=:\n"
    "v_fma_f32 v224, -v224, v253, v224\n"
    "v_pk_mul_f32 v[16:17], v[16:17], v[224:225] op_sel_hi:[1,0]\n"
    "v_pk_mul_f32 v[18:19], v[18:19], v[224:225] op_sel_hi:[1,0]\n"
    "v_pk_mul_f32 v[20:21], v[20:21], v[224:225] op_sel_hi:[1,0]\n"
    "v_pk_mul_f32 v[22:23], v[22:23], v[224:225] op_sel_hi:[1,0]\n"
    "v_cvt_pk_bf16_f32 v224, v16, v17\n"
    "v_cvt_pk_bf16_f32 v225, v18, v19\n"
    "v_cvt_pk_bf16_f32 v226, v20, v21\n"
    "v_cvt_pk_bf16_f32 v227, v22, v23\n"
    "v_min_u32 v224, -2, v224\n"
    "v_min_u32 v225, -2, v225\n"
    "v_min_u32 v226, -2, v226\n"
    "v_min_u32 v227, -2, v227\n"
    "buffer_store_dwordx4 v[224:227], v237, s[12:15], s20 offen sc1\n"
    "s_add_u32 s98, s18, 32768\n"
    "buffer_load_dwordx4 v[32:35], v237, s[88:91], s98 offen sc1\n"
    "buffer_load_dwordx4 v[36:39], v237, s[88:91], s98 offen offset:1024 sc1\n"
    "s_add_u32 s98, s19, 32768\n"
    "buffer_load_dwordx4 v[40:43], v237, s[88:91], s98 offen sc1\n"
    "buffer_load_dwordx4 v[44:47], v237, s[88:91], s98 offen offset:1024 sc1\n"
    "s_add_u32 s98, s22, 32768\n"
    "buffer_load_dwordx4 v[48:51], v237, s[88:91], s98 offen sc1\n"
    "buffer_load_dwordx4 v[52:55], v237, s[88:91], s98 offen offset:1024 sc1\n"
    "s_add_u32 s98, s23, 32768\n"
    "buffer_load_dwordx4 v[56:59], v237, s[88:91], s98 offen sc1\n"
    "buffer_load_dwordx4 v[60:63], v237, s[88:91], s98 offen offset:1024 sc1\n"
    "s_waitcnt vmcnt(23)\n"
    "L_ckA0%=:\n"
    "v_max3_u32 v199, v128, v129, v130\n"
    "v_max3_u32 v199, v199, v131, v160\n"
    "v_max3_u32 v199, v199, v161, v162\n"
    "v_max_u32 v199, v199, v163\n"
    "v_cmp_eq_u32 vcc, -1, v199\n"
    "s_nop 0\n"
    "s_cmp_lg_u64 vcc, 0\n"
    "s_cbranch_scc1 L_reA0%=\n"
    "L_goA0%=:\n"
    "s_waitcnt lgkmcnt(4)\n"
    "v_mfma_f32_32x32x16_bf16 v[64:79], a[104:107], v[128:131], 0\n"
    "ds_read_b64_tr_b16 a[120:121], v241 offset:16384\n"
    "ds_read_b64_tr_b16 a[122:123], v241 offset:17408\n"
    "v_mfma_f32_32x32x16_bf16 v[0:15], a[104:107], v[160:163], 0\n"
    "ds_read_b64_tr_b16 a[124:125], v242 offset:16384\n"
    "ds_read_b64_tr_b16 a[126:127], v242 offset:17408\n"
    "v_mfma_f32_32x32x16_bf16 v[80:95], a[108:111], v[128:131], 0\n"
    "v_lshrrev_b32 v200, 4, v237\n"
    "v_and_b32 v201, 7, v200\n"
    "v_mfma_f32_32x32x16_bf16 v[16:31], a[108:111], v[160:163], 0\n"
    "v_lshrrev_b32 v202, 5, v200\n"
    "s_waitcnt vmcnt(22)\n"
    "L_ckA1%=:\n"
    "v_max3_u32 v199, v132, v133, v134\n"
    "v_max3_u32 v199, v199, v135, v164\n"
    "v_max3_u32 v199, v199, v165, v166\n"
    "v_max_u32 v199, v199, v167\n"
    "v_cmp_eq_u32 vcc, -1, v199\n"
    "s_nop 0\n"
    "s_cmp_lg_u64 vcc, 0\n"
    "s_cbranch_scc1 L_reA1%=\n"
    "L_goA1%=:\n"
    "s_waitcnt lgkmcnt(4)\n"
    "v_mfma_f32_32x32x16_bf16 v[64:79], a[112:115], v[132:135], v[64:79]\n"
    "ds_read_b64_tr_b16 a[128:129], v241 offset:18432\n"
    "ds_read_b64_tr_b16 a[130:131], v241 offset:19456\n"
    "ds_read_b64_tr_b16 a[132:133], v242 offset:18432\n"
    "v_mfma_f32_32x32x16_bf16 v[0:15], a[112:115], v[164:167], v[0:15]\n"
    "ds_read_b64_tr_b16 a[134:135], v242 offset:19456\n"
    "v_xor_b32 v202, v202, v201\n"
    "v_mfma_f32_32x32x16_bf16 v[80:95], a[116:119], v[132:135], v[80:95]\n"
    "v_and_b32 v203, 15, v200\n"
    "v_lshlrev_b32 v203, 8, v203\n"
    "v_lshl_add_u32 v203, v202, 4, v203\n"
    "v_mfma_f32_32x32x16_bf16 v[16:31], a[116:119], v[164:167], v[16:31]\n"
    "v_bfe_u32 v202, v200, 4, 1\n"
    "s_waitcnt vmcnt(19)\n"
    "L_ckA2%=:\n"
    "v_max3_u32 v199, v136, v137, v138\n"
    "v_max3_u32 v199, v199, v139, v168\n"
    "v_max3_u32 v199, v199, v169, v170\n"
    "v_max_u32 v199, v199, v171\n"
    "v_cmp_eq_u32 vcc, -1, v199\n"
    "s_nop 0\n"
    "s_cmp_lg_u64 vcc, 0\n"
    "s_cbranch_scc1 L_reA2%=\n"
    "L_goA2%=:\n"
    "s_waitcnt lgkmcnt(4)\n"
    "v_mfma_f32_32x32x16_bf16 v[64:79], a[120:123], v[136:139], v[64:79]\n"
    "ds_read_b64_tr_b16 a[136:137], v241 offset:32768\n"
    "ds_read_b64_tr_b16 a[138:139], v241 offset:33792\n"
    "ds_read_b64_tr_b16 a[140:141], v242 offset:32768\n"
    "v_mfma_f32_32x32x16_bf16 v[0:15], a[120:123], v[168:171], v[0:15]\n"
    "ds_read_b64_tr_b16 a[142:143], v242 offset:33792\n"
    "v_lshl_add_u32 v203, v202, 14, v203\n"
    "v_mfma_f32_32x32x16_bf16 v[80:95], a[124:127], v[136:139], v[80:95]\n"
    "v_add_u32 v192, s21, v203\n"
    "v_xor_b32 v193, 32, v192\n"
    "v_xor_b32 v194, 64, v192\n"
    "v_mfma_f32_32x32x16_bf16 v[16:31], a[124:127], v[168:171], v[16:31]\n"
    "v_xor_b32 v195, 0x60, v192\n"
    "s_waitcnt vmcnt(18)\n"
    "L_ckA3%=:\n"
    "v_max3_u32 v199, v140, v141, v142\n"
    "v_max3_u32 v199, v199, v143, v172\n"
    "v_max3_u32 v199, v199, v173, v174\n"
    "v_max_u32 v199, v199, v175\n"
    "v_cmp_eq_u32 vcc, -1, v199\n"
    "s_nop 0\n"
    "s_cmp_lg_u64 vcc, 0\n"
    "s_cbranch_scc1 L_reA3%=\n"
    "L_goA3%=:\n"
    "s_waitcnt lgkmcnt(4)\n"
    "v_mfma_f32_32x32x16_bf16 v[64:79], a[128:131], v[140:143], v[64:79]\n"
    "ds_read_b64_tr_b16 a[144:145], v241 offset:34816\n"
    "ds_read_b64_tr_b16 a[146:147], v241 offset:35840\n"
    "ds_read_b64_tr_b16 a[148:149], v242 offset:34816\n"
    "v_mfma_f32_32x32x16_bf16 v[0:15], a[128:131], v[172:175], v[0:15]\n"
    "ds_read_b64_tr_b16 a[150:151], v242 offset:35840\n"
    "v_lshrrev_b32 v202, 3, v200\n"
    "v_mfma_f32_32x32x16_bf16 v[80:95], a[132:135], v[140:143], v[80:95]\n"
    "v_lshlrev_b32 v203, 1, v201\n"
    "v_xor_b32 v203, v203, v202\n"
    "v_lshlrev_b32 v203, 4, v203\n"
    "v_mfma_f32_32x32x16_bf16 v[16:31], a[132:135], v[172:175], v[16:31]\n"
    "v_lshl_add_u32 v203, v202, 8, v203\n"
    "s_waitcnt vmcnt(14)\n"
    "L_ckA4%=:\n"
    "v_max3_u32 v199, v144, v145, v146\n"
    "v_max3_u32 v199, v199, v147, v176\n"
    "v_max3_u32 v199, v199, v177, v178\n"
    "v_max_u32 v199, v199, v179\n"
    "v_cmp_eq_u32 vcc, -1, v199\n"
    "s_nop 0\n"
    "s_cmp_lg_u64 vcc, 0\n"
    "s_cbranch_scc1 L_reA4%=\n"
    "L_goA4%=:\n"
    "s_waitcnt lgkmcnt(4)\n"
    "v_mfma_f32_32x32x16_bf16 v[64:79], a[136:139], v[144:147], v[64:79]\n"
    "ds_read_b64_tr_b16 a[104:105], v241 offset:49152\n"
    "ds_read_b64_tr_b16 a[106:107], v241 offset:50176\n"
    "ds_read_b64_tr_b16 a[108:109], v242 offset:49152\n"
    "ds_read_b64_tr_b16 a[110:111], v242 offset:50176\n"
    "v_mfma_f32_32x32x16_bf16 v[0:15], a[136:139], v[176:179], v[0:15]\n"
    "s_add_u32 s98, s18, 98304\n"
    "buffer_load_dwordx4 v[96:99], v237, s[88:91], s98 offen sc1\n"
    "buffer_load_dwordx4 v[100:103], v237, s[88:91], s98 offen offset:1024 sc1\n"
    "s_add_u32 s98, s19, 98304\n"
    "buffer_load_dwordx4 v[104:107], v237, s[88:91], s98 offen sc1\n"
    "buffer_load_dwordx4 v[108:111], v237, s[88:91], s98 offen offset:1024 sc1\n"
    "s_add_u32 s98, s22, 98304\n"
    "buffer_load_dwordx4 v[112:115], v237, s[88:91], s98 offen sc1\n"
    "buffer_load_dwordx4 v[116:119], v237, s[88:91], s98 offen offset:1024 sc1\n"
    "v_mfma_f32_32x32x16_bf16 v[80:95], a[140:143], v[144:147], v[80:95]\n"
    "s_add_u32 s98, s23, 98304\n"
    "buffer_load_dwordx4 v[120:123], v237, s[88:91], s98 offen sc1\n"
    "buffer_load_dwordx4 v[124:127], v237, s[88:91], s98 offen offset:1024 sc1\n"
    "s_lshr_b32 s35, s11, 1\n"
    "s_lshl_b32 s35, s35, 14\n"
    "s_and_b32 s63, s11, 1\n"
    "v_mfma_f32_32x32x16_bf16 v[16:31], a[140:143], v[176:179], v[16:31]\n"
    "s_lshl_b32 s63, s63, 11\n"
    "s_add_u32 s35, s35, s63\n"
    "s_waitcnt vmcnt(21)\n"
    "L_ckA5%=:\n"
    "v_max3_u32 v199, v148, v149, v150\n"
    "v_max3_u32 v199, v199, v151, v180\n"
    "v_max3_u32 v199, v199, v181, v182\n"
    "v_max_u32 v199, v199, v183\n"
    "v_cmp_eq_u32 vcc, -1, v199\n"
    "s_nop 0\n"
    "s_cmp_lg_u64 vcc, 0\n"
    "s_cbranch_scc1 L_reA5%=\n"
    "L_goA5%=:\n"
    "s_waitcnt lgkmcnt(4)\n"
    "v_mfma_f32_32x32x16_bf16 v[64:79], a[144:147], v[148:151], v[64:79]\n"
    "ds_read_b64_tr_b16 a[112:113], v241 offset:51200\n"
    "ds_read_b64_tr_b16 a[114:115], v241 offset:52224\n"
    "ds_read_b64_tr_b16 a[116:117], v242 offset:51200\n"
    "v_mfma_f32_32x32x16_bf16 v[0:15], a[144:147], v[180:183], v[0:15]\n"
    "ds_read_b64_tr_b16 a[118:119], v242 offset:52224\n"
    "v_add_u32 v241, 0x10000, v241\n"
    "v_add_u32 v242, 0x10000, v242\n"
    "s_add_u32 s35, s35, 32768\n"
    "v_mfma_f32_32x32x16_bf16 v[80:95], a[148:151], v[148:151], v[80:95]\n"
    "v_add_u32 v196, s35, v203\n"
    "v_xor_b32 v197, 16, v196\n"
    "v_lshlrev_b32 v202, 10, v202\n"
    "v_mfma_f32_32x32x16_bf16 v[16:31], a[148:151], v[180:183], v[16:31]\n"
    "v_lshl_add_u32 v198, v201, 4, v202\n"
    "s_waitcnt vmcnt(18)\n"
    "L_ckA6%=:\n"
    "v_max3_u32 v199, v152, v153, v154\n"
    "v_max3_u32 v199, v199, v155, v184\n"
    "v_max3_u32 v199, v199, v185, v186\n"
    "v_max_u32 v199, v199, v187\n"
    "v_cmp_eq_u32 vcc, -1, v199\n"
    "s_nop 0\n"
    "s_cmp_lg_u64 vcc, 0\n"
    "s_cbranch_scc1 L_reA6%=\n"
    "L_goA6%=:\n"
    "s_waitcnt lgkmcnt(4)\n"
    "v_mfma_f32_32x32x16_bf16 v[64:79], a[104:107], v[152:155], v[64:79]\n"
    "ds_read_b64_tr_b16 a[0:1], v241 offset:0\n"
    "ds_read_b64_tr_b16 a[2:3], v241 offset:1024\n"
    "v_mfma_f32_32x32x16_bf16 v[0:15], a[104:107], v[184:187], v[0:15]\n"
    "ds_read_b64_tr_b16 a[4:5], v242 offset:0\n"
    "ds_read_b64_tr_b16 a[6:7], v242 offset:1024\n"
    "v_mfma_f32_32x32x16_bf16 v[80:95], a[108:111], v[152:155], v[80:95]\n"
    "ds_read_b64_tr_b16 a[8:9], v241 offset:2048\n"
    "ds_read_b64_tr_b16 a[10:11], v241 offset:3072\n"
    "v_mfma_f32_32x32x16_bf16 v[16:31], a[108:111], v[184:187], v[16:31]\n"
    "s_waitcnt vmcnt(17)\n"
    "L_ckA7%=:\n"
    "v_max3_u32 v199, v156, v157, v158\n"
    "v_max3_u32 v199, v199, v159, v188\n"
    "v_max3_u32 v199, v199, v189, v190\n"
    "v_max_u32 v199, v199, v191\n"
    "v_cmp_eq_u32 vcc, -1, v199\n"
    "s_nop 0\n"
    "s_cmp_lg_u64 vcc, 0\n"
    "s_cbranch_scc1 L_reA7%=\n"
    "L_goA7%=:\n"
    "s_waitcnt lgkmcnt(6)\n"
    "v_mfma_f32_32x32x16_bf16 v[64:79], a[112:115], v[156:159], v[64:79]\n"
    "ds_read_b64_tr_b16 a[12:13], v242 offset:2048\n"
    "ds_read_b64_tr_b16 a[14:15], v242 offset:3072\n"
    "v_mfma_f32_32x32x16_bf16 v[0:15], a[112:115], v[188:191], v[0:15]\n"
    "ds_read_b64_tr_b16 a[16:17], v241 offset:16384\n"
    "ds_read_b64_tr_b16 a[18:19], v241 offset:17408\n"
    "v_mfma_f32_32x32x16_bf16 v[80:95], a[116:119], v[156:159], v[80:95]\n"
    "ds_read_b64_tr_b16 a[20:21], v242 offset:16384\n"
    "ds_read_b64_tr_b16 a[22:23], v242 offset:17408\n"
    "v_mfma_f32_32x32x16_bf16 v[16:31], a[116:119], v[188:191], v[16:31]\n"
    "s_waitcnt vmcnt(14)\n"
    "L_ckB00%=:\n"
    "v_max3_u32 v199, v32, v33, v34\n"
    "v_max3_u32 v199, v199, v35, v36\n"
    "v_max3_u32 v199, v199, v37, v38\n"
    "v_max_u32 v199, v199, v39\n"
    "v_cmp_eq_u32 vcc, -1, v199\n"
    "s_nop 0\n"
    "s_cmp_lg_u64 vcc, 0\n"
    "s_cbranch_scc1 L_reB00%=\n"
    "L_goB00%=:\n"
    "s_waitcnt lgkmcnt(8)\n"
    "v_mfma_f32_32x32x16_bf16 v[64:79], a[0:3], v[32:35], v[64:79]\n"
    "ds_read_b64_tr_b16 a[24:25], v241 offset:18432\n"
    "ds_read_b64_tr_b16 a[26:27], v241 offset:19456\n"
    "ds_read_b64_tr_b16 a[28:29], v242 offset:18432\n"
    "v_mfma_f32_32x32x16_bf16 v[80:95], a[4:7], v[32:35], v[80:95]\n"
    "ds_read_b64_tr_b16 a[30:31], v242 offset:19456\n"
    "ds_read_b64_tr_b16 a[32:33], v241 offset:32768\n"
    "s_waitcnt lgkmcnt(9)\n"
    "v_mfma_f32_32x32x16_bf16 v[64:79], a[8:11], v[36:39], v[64:79]\n"
    "ds_read_b64_tr_b16 a[34:35], v241 offset:33792\n"
    "ds_read_b64_tr_b16 a[36:37], v242 offset:32768\n"
    "v_mfma_f32_32x32x16_bf16 v[80:95], a[12:15], v[36:39], v[80:95]\n"
    "ds_read_b64_tr_b16 a[38:39], v242 offset:33792\n"
    "s_waitcnt vmcnt(12)\n"
    "L_ckB01%=:\n"
    "v_max3_u32 v199, v40, v41, v42\n"
    "v_max3_u32 v199, v199, v43, v44\n"
    "v_max3_u32 v199, v199, v45, v46\n"
    "v_max_u32 v199, v199, v47\n"
    "v_cmp_eq_u32 vcc, -1, v199\n"
    "s_nop 0\n"
    "s_cmp_lg_u64 vcc, 0\n"
    "s_cbranch_scc1 L_reB01%=\n"
    "L_goB01%=:\n"
    "s_waitcnt lgkmcnt(8)\n"
    "v_mfma_f32_32x32x16_bf16 v[64:79], a[16:19], v[40:43], v[64:79]\n"
    "ds_read_b64_tr_b16 a[40:41], v241 offset:34816\n"
    "ds_read_b64_tr_b16 a[42:43], v241 offset:35840\n"
    "ds_read_b64_tr_b16 a[44:45], v242 offset:34816\n"
    "v_mfma_f32_32x32x16_bf16 v[80:95], a[20:23], v[40:43], v[80:95]\n"
    "ds_read_b64_tr_b16 a[46:47], v242 offset:35840\n"
    "ds_read_b64_tr_b16 a[48:49], v241 offset:49152\n"
    "s_waitcnt lgkmcnt(9)\n"
    "v_mfma_f32_32x32x16_bf16 v[64:79], a[24:27], v[44:47], v[64:79]\n"
    "ds_read_b64_tr_b16 a[50:51], v241 offset:50176\n"
    "ds_read_b64_tr_b16 a[52:53], v242 offset:49152\n"
    "v_mfma_f32_32x32x16_bf16 v[80:95], a[28:31], v[44:47], v[80:95]\n"
    "ds_read_b64_tr_b16 a[54:55], v242 offset:50176\n"
    "s_waitcnt vmcnt(10)\n"
    "L_ckB02%=:\n"
    "v_max3_u32 v199, v48, v49, v50\n"
    "v_max3_u32 v199, v199, v51, v52\n"
    "v_max3_u32 v199, v199, v53, v54\n"
    "v_max_u32 v199, v199, v55\n"
    "v_cmp_eq_u32 vcc, -1, v199\n"
    "s_nop 0\n"
    "s_cmp_lg_u64 vcc, 0\n"
    "s_cbranch_scc1 L_reB02%=\n"
    "L_goB02%=:\n"
    "s_waitcnt lgkmcnt(8)\n"
    "v_mfma_f32_32x32x16_bf16 v[64:79], a[32:35], v[48:51], v[64:79]\n"
    "ds_read_b64_tr_b16 a[56:57], v241 offset:51200\n"
    "ds_read_b64_tr_b16 a[58:59], v241 offset:52224\n"
    "v_mfma_f32_32x32x16_bf16 v[80:95], a[36:39], v[48:51], v[80:95]\n"
    "ds_read_b64_tr_b16 a[60:61], v242 offset:51200\n"
    "s_waitcnt lgkmcnt(7)\n"
    "v_mfma_f32_32x32x16_bf16 v[64:79], a[40:43], v[52:55], v[64:79]\n"
    "ds_read_b64_tr_b16 a[62:63], v242 offset:52224\n"
    "v_mfma_f32_32x32x16_bf16 v[80:95], a[44:47], v[52:55], v[80:95]\n"
    "s_waitcnt vmcnt(8)\n"
    "L_ckB03%=:\n"
    "v_max3_u32 v199, v56, v57, v58\n"
    "v_max3_u32 v199, v199, v59, v60\n"
    "v_max3_u32 v199, v199, v61, v62\n"
    "v_max_u32 v199, v199, v63\n"
    "v_cmp_eq_u32 vcc, -1, v199\n"
    "s_nop 0\n"
    "s_cmp_lg_u64 vcc, 0\n"
    "s_cbranch_scc1 L_reB03%=\n"
    "L_goB03%=:\n"
    "s_waitcnt lgkmcnt(4)\n"
    "v_mfma_f32_32x32x16_bf16 v[64:79], a[48:51], v[56:59], v[64:79]\n"
    "v_mfma_f32_32x32x16_bf16 v[80:95], a[52:55], v[56:59], v[80:95]\n"
    "s_waitcnt lgkmcnt(0)\n"
    "v_mfma_f32_32x32x16_bf16 v[64:79], a[56:59], v[60:63], v[64:79]\n"
    "v_mfma_f32_32x32x16_bf16 v[80:95], a[60:63], v[60:63], v[80:95]\n"
    "s_nop 10\n"
    "ds_write_b128 v192, v[64:67] offset:0\n"
    "ds_write_b128 v193, v[68:71] offset:0\n"
    "ds_write_b128 v194, v[72:75] offset:0\n"
    "ds_write_b128 v195, v[76:79] offset:0\n"
    "ds_write_b128 v192, v[80:83] offset:128\n"
    "ds_write_b128 v193, v[84:87] offset:128\n"
    "ds_write_b128 v194, v[88:91] offset:128\n"
    "ds_write_b128 v195, v[92:95] offset:128\n"
    "s_waitcnt lgkmcnt(0)\n"
    "s_barrier\n"
    "ds_read_b128 v[128:131], v196 offset:0\n"
    "ds_read_b128 v[132:135], v197 offset:0\n"
    "ds_read_b128 v[136:139], v196 offset:4096\n"
    "ds_read_b128 v[140:143], v197 offset:4096\n"
    "ds_read_b128 v[144:147], v196 offset:8192\n"
    "ds_read_b128 v[148:151], v197 offset:8192\n"
    "ds_read_b128 v[152:155], v196 offset:12288\n"
    "ds_read_b128 v[156:159], v197 offset:12288\n"
    "s_mov_b64 exec, 0x30\n"
    "global_store_dwordx2 v246, v[244:245], s[26:27] sc1\n"
    "s_mov_b64 exec, -1\n"
    "s_waitcnt vmcnt(7)\n"
    "L_ckB10%=:\n"
    "v_max3_u32 v199, v96, v97, v98\n"
    "v_max3_u32 v199, v199, v99, v100\n"
    "v_max3_u32 v199, v199, v101, v102\n"
    "v_max_u32 v199, v199, v103\n"
    "v_cmp_eq_u32 vcc, -1, v199\n"
    "s_nop 0\n"
    "s_cmp_lg_u64 vcc, 0\n"
    "s_cbranch_scc1 L_reB10%=\n"
    "L_goB10%=:\n"
    "v_mfma_f32_32x32x16_bf16 v[0:15], a[0:3], v[96:99], v[0:15]\n"
    "s_waitcnt lgkmcnt(4)\n"
    "v_pk_add_f32 v[128:129], v[128:129], v[136:137]\n"
    "v_pk_add_f32 v[130:131], v[130:131], v[138:139]\n"
    "v_mfma_f32_32x32x16_bf16 v[16:31], a[4:7], v[96:99], v[16:31]\n"
    "v_pk_add_f32 v[132:133], v[132:133], v[140:141]\n"
    "v_mfma_f32_32x32x16_bf16 v[0:15], a[8:11], v[100:103], v[0:15]\n"
    "v_pk_add_f32 v[134:135], v[134:135], v[142:143]\n"
    "s_waitcnt lgkmcnt(2)\n"
    "v_pk_add_f32 v[128:129], v[128:129], v[144:145]\n"
    "v_mfma_f32_32x32x16_bf16 v[16:31], a[12:15], v[100:103], v[16:31]\n"
    "s_waitcnt vmcnt(5)\n"
    "L_ckB11%=:\n"
    "v_max3_u32 v199, v104, v105, v106\n"
    "v_max3_u32 v199, v199, v107, v108\n"
    "v_max3_u32 v199, v199, v109, v110\n"
    "v_max_u32 v199, v199, v111\n"
    "v_cmp_eq_u32 vcc, -1, v199\n"
    "s_nop 0\n"
    "s_cmp_lg_u64 vcc, 0\n"
    "s_cbranch_scc1 L_reB11%=\n"
    "L_goB11%=:\n"
    "v_mfma_f32_32x32x16_bf16 v[0:15], a[16:19], v[104:107], v[0:15]\n"
    "v_pk_add_f32 v[130:131], v[130:131], v[146:147]\n"
    "v_pk_add_f32 v[132:133], v[132:133], v[148:149]\n"
    "v_mfma_f32_32x32x16_bf16 v[16:31], a[20:23], v[104:107], v[16:31]\n"
    "v_pk_add_f32 v[134:135], v[134:135], v[150:151]\n"
    "v_mfma_f32_32x32x16_bf16 v[0:15], a[24:27], v[108:111], v[0:15]\n"
    "s_waitcnt lgkmcnt(0)\n"
    "v_pk_add_f32 v[128:129], v[128:129], v[152:153]\n"
    "v_mfma_f32_32x32x16_bf16 v[16:31], a[28:31], v[108:111], v[16:31]\n"
    "s_waitcnt vmcnt(3)\n"
    "L_ckB12%=:\n"
    "v_max3_u32 v199, v112, v113, v114\n"
    "v_max3_u32 v199, v199, v115, v116\n"
    "v_max3_u32 v199, v199, v117, v118\n"
    "v_max_u32 v199, v199, v119\n"
    "v_cmp_eq_u32 vcc, -1, v199\n"
    "s_nop 0\n"
    "s_cmp_lg_u64 vcc, 0\n"
    "s_cbranch_scc1 L_reB12%=\n"
    "L_goB12%=:\n"
    "v_mfma_f32_32x32x16_bf16 v[0:15], a[32:35], v[112:115], v[0:15]\n"
    "v_pk_add_f32 v[130:131], v[130:131], v[154:155]\n"
    "v_pk_add_f32 v[132:133], v[132:133], v[156:157]\n"
    "v_mfma_f32_32x32x16_bf16 v[16:31], a[36:39], v[112:115], v[16:31]\n"
    "v_pk_add_f32 v[134:135], v[134:135], v[158:159]\n"
    "v_mfma_f32_32x32x16_bf16 v[0:15], a[40:43], v[116:119], v[0:15]\n"
    "v_cvt_pk_bf16_f32 v160, v128, v129\n"
    "v_mfma_f32_32x32x16_bf16 v[16:31], a[44:47], v[116:119], v[16:31]\n"
    "s_waitcnt vmcnt(1)\n"
    "L_ckB13%=:\n"
    "v_max3_u32 v199, v120, v121, v122\n"
    "v_max3_u32 v199, v199, v123, v124\n"
    "v_max3_u32 v199, v199, v125, v126\n"
    "v_max_u32 v199, v199, v127\n"
    "v_cmp_eq_u32 vcc, -1, v199\n"
    "s_nop 0\n"
    "s_cmp_lg_u64 vcc, 0\n"
    "s_cbranch_scc1 L_reB13%=\n"
    "L_goB13%=:\n"
    "v_mfma_f32_32x32x16_bf16 v[0:15], a[48:51], v[120:123], v[0:15]\n"
    "v_cvt_pk_bf16_f32 v161, v130, v131\n"
    "v_mfma_f32_32x32x16_bf16 v[16:31], a[52:55], v[120:123], v[16:31]\n"
    "v_cvt_pk_bf16_f32 v162, v132, v133\n"
    "v_mfma_f32_32x32x16_bf16 v[0:15], a[56:59], v[124:127], v[0:15]\n"
    "v_cvt_pk_bf16_f32 v163, v134, v135\n"
    "v_mfma_f32_32x32x16_bf16 v[16:31], a[60:63], v[124:127], v[16:31]\n"
    "global_store_dwordx4 v198, v[160:163], s[86:87]\n"
    "s_nop 8\n"
    "ds_write_b128 v192, v[0:3] offset:32768\n"
    "ds_write_b128 v193, v[4:7] offset:32768\n"
    "ds_write_b128 v194, v[8:11] offset:32768\n"
    "ds_write_b128 v195, v[12:15] offset:32768\n"
    "ds_write_b128 v192, v[16:19] offset:32896\n"
    "ds_write_b128 v193, v[20:23] offset:32896\n"
    "ds_write_b128 v194, v[24:27] offset:32896\n"
    "ds_write_b128 v195, v[28:31] offset:32896\n"
    "s_waitcnt lgkmcnt(0)\n"
    "s_barrier\n"
    "ds_read_b128 v[128:131], v196 offset:32768\n"
    "ds_read_b128 v[132:135], v197 offset:32768\n"
    "ds_read_b128 v[136:139], v196 offset:36864\n"
    "ds_read_b128 v[140:143], v197 offset:36864\n"
    "ds_read_b128 v[144:147], v196 offset:40960\n"
    "ds_read_b128 v[148:151], v197 offset:40960\n"
    "ds_read_b128 v[152:155], v196 offset:45056\n"
    "ds_read_b128 v[156:159], v197 offset:45056\n"
    "s_waitcnt lgkmcnt(4)\n"
    "v_pk_add_f32 v[128:129], v[128:129], v[136:137]\n"
    "v_pk_add_f32 v[130:131], v[130:131], v[138:139]\n"
    "v_pk_add_f32 v[132:133], v[132:133], v[140:141]\n"
    "v_pk_add_f32 v[134:135], v[134:135], v[142:143]\n"
    "s_waitcnt lgkmcnt(2)\n"
    "v_pk_add_f32 v[128:129], v[128:129], v[144:145]\n"
    "v_pk_add_f32 v[130:131], v[130:131], v[146:147]\n"
    "v_pk_add_f32 v[132:133], v[132:133], v[148:149]\n"
    "v_pk_add_f32 v[134:135], v[134:135], v[150:151]\n"
    "s_waitcnt lgkmcnt(0)\n"
    "v_pk_add_f32 v[128:129], v[128:129], v[152:153]\n"
    "v_pk_add_f32 v[130:131], v[130:131], v[154:155]\n"
    "v_pk_add_f32 v[132:133], v[132:133], v[156:157]\n"
    "v_pk_add_f32 v[134:135], v[134:135], v[158:159]\n"
    "v_cvt_pk_bf16_f32 v164, v128, v129\n"
    "v_cvt_pk_bf16_f32 v165, v130, v131\n"
    "v_cvt_pk_bf16_f32 v166, v132, v133\n"
    "v_cvt_pk_bf16_f32 v167, v134, v135\n"
    "v_add_u32 v198, 0x8000, v198\n"
    "global_store_dwordx4 v198, v[164:167], s[86:87]\n"
    "s_endpgm\n"
    "L_slow_hpoll%=:\n"
    "global_load_dwordx2 v[248:249], v250, s[26:27] sc1\n"
    "s_add_u32 s92, s92, 1\n"
    "s_cmp_gt_u32 s92, 0x40000\n"
    "s_waitcnt vmcnt(0)\n"
    "s_cbranch_scc1 L_ok_hpoll%=\n"
    "v_cmp_eq_u64 vcc, s[64:65], v[248:249]\n"
    "s_nop 0\n"
    "s_and_b64 vcc, vcc, s[74:75]\n"
    "s_cmp_eq_u64 vcc, s[74:75]\n"
    "s_cbranch_scc1 L_ok_hpoll%=\n"
    "s_branch L_slow_hpoll%=\n"
    "L_slow_cpoll%=:\n"
    "global_load_dwordx2 v[248:249], v251, s[26:27] sc1\n"
    "s_add_u32 s92, s92, 1\n"
    "s_cmp_gt_u32 s92, 0x40000\n"
    "s_waitcnt vmcnt(0)\n"
    "s_cbranch_scc1 L_ok_cpoll%=\n"
    "v_cmp_eq_u64 vcc, s[66:67], v[248:249]\n"
    "s_nop 0\n"
    "s_cmp_eq_u64 vcc, -1\n"
    "s_cbranch_scc1 L_ok_cpoll%=\n"
    "s_branch L_slow_cpoll%=\n"
    "L_fb01%=:\n"
    "global_store_dwordx4 v237, v[160:163], s[84:85] offset:-2048 sc1\n"
    "global_store_dwordx4 v237, v[164:167], s[84:85] offset:-1024 sc1\n"
    "s_mov_b64 exec, s[70:71]\n"
    "global_store_dwordx4 v237, v[184:187], s[84:85] offset:0 sc1\n"
    "global_store_dwordx4 v237, v[188:191], s[84:85] offset:1024 sc1\n"
    "global_store_dwordx4 v237, v[192:195], s[84:85] offset:2048 sc1\n"
    "s_mov_b64 exec, -1\n"
    "s_waitcnt vmcnt(0)\n"
    "v_accvgpr_write_b32 a152, v212\n"
    "v_accvgpr_write_b32 a153, v213\n"
    "v_accvgpr_write_b32 a154, v214\n"
    "v_accvgpr_write_b32 a155, v215\n"
    "s_lshl_b32 s33, s28, 1\n"
    "s_add_u32 s33, s33, 1\n"
    "s_mov_b64 s[96:97], s[70:71]\n"
    "s_mov_b32 s98, 15\n"
    "s_mov_b32 s99, 1\n"
    "s_mov_b32 s60, 2048\n"
    "s_mov_b32 s62, 1\n"
    "s_getpc_b64 s[94:95]\n"
    "s_add_u32 s94, s94, 12\n"
    "s_addc_u32 s95, s95, 0\n"
    "s_branch L_FB%=\n"
    "v_accvgpr_read_b32 v48, a168\n"
    "v_accvgpr_read_b32 v49, a169\n"
    "v_accvgpr_read_b32 v50, a170\n"
    "v_accvgpr_read_b32 v51, a171\n"
    "v_accvgpr_read_b32 v52, a172\n"
    "v_accvgpr_read_b32 v53, a173\n"
    "v_accvgpr_read_b32 v54, a174\n"
    "v_accvgpr_read_b32 v55, a175\n"
    "v_accvgpr_read_b32 v56, a176\n"
    "v_accvgpr_read_b32 v57, a177\n"
    "v_accvgpr_read_b32 v58, a178\n"
    "v_accvgpr_read_b32 v59, a179\n"
    "v_accvgpr_read_b32 v60, a180\n"
    "v_accvgpr_read_b32 v61, a181\n"
    "v_accvgpr_read_b32 v62, a182\n"
    "v_accvgpr_read_b32 v63, a183\n"
    "v_rcp_f32 v224, v48\n"
    "s_nop 1\n"
    "s_branch L_norm01%=\n"
    "L_fb00%=:\n"
    "global_store_dwordx4 v237, v[160:163], s[84:85] offset:-2048 sc1\n"
    "global_store_dwordx4 v237, v[164:167], s[84:85] offset:-1024 sc1\n"
    "s_mov_b64 exec, s[70:71]\n"
    "global_store_dwordx4 v237, v[184:187], s[84:85] offset:0 sc1\n"
    "global_store_dwordx4 v237, v[188:191], s[84:85] offset:1024 sc1\n"
    "global_store_dwordx4 v237, v[192:195], s[84:85] offset:2048 sc1\n"
    "s_mov_b64 exec, -1\n"
    "s_waitcnt vmcnt(0)\n"
    "v_accvgpr_write_b32 a152, v208\n"
    "v_accvgpr_write_b32 a153, v209\n"
    "v_accvgpr_write_b32 a154, v210\n"
    "v_accvgpr_write_b32 a155, v211\n"
    "s_lshl_b32 s33, s28, 1\n"
    "s_add_u32 s33, s33, 0\n"
    "s_mov_b64 s[96:97], s[70:71]\n"
    "s_mov_b32 s98, 15\n"
    "s_mov_b32 s99, 1\n"
    "s_mov_b32 s60, 2048\n"
    "s_mov_b32 s62, 1\n"
    "s_getpc_b64 s[94:95]\n"
    "s_add_u32 s94, s94, 12\n"
    "s_addc_u32 s95, s95, 0\n"
    "s_branch L_FB%=\n"
    "v_accvgpr_read_b32 v32, a168\n"
    "v_accvgpr_read_b32 v33, a169\n"
    "v_accvgpr_read_b32 v34, a170\n"
    "v_accvgpr_read_b32 v35, a171\n"
    "v_accvgpr_read_b32 v36, a172\n"
    "v_accvgpr_read_b32 v37, a173\n"
    "v_accvgpr_read_b32 v38, a174\n"
    "v_accvgpr_read_b32 v39, a175\n"
    "v_accvgpr_read_b32 v40, a176\n"
    "v_accvgpr_read_b32 v41, a177\n"
    "v_accvgpr_read_b32 v42, a178\n"
    "v_accvgpr_read_b32 v43, a179\n"
    "v_accvgpr_read_b32 v44, a180\n"
    "v_accvgpr_read_b32 v45, a181\n"
    "v_accvgpr_read_b32 v46, a182\n"
    "v_accvgpr_read_b32 v47, a183\n"
    "v_rcp_f32 v224, v32\n"
    "s_nop 1\n"
    "s_branch L_norm00%=\n"
    "L_fb10%=:\n"
    "v_accvgpr_write_b32 a152, v200\n"
    "v_accvgpr_write_b32 a153, v201\n"
    "v_accvgpr_write_b32 a154, v202\n"
    "v_accvgpr_write_b32 a155, v203\n"
    "s_lshl_b32 s33, s28, 1\n"
    "s_add_u32 s33, s33, 0\n"
    "s_mov_b64 s[96:97], s[68:69]\n"
    "s_mov_b32 s98, 256\n"
    "s_mov_b32 s99, 0\n"
    "s_mov_b32 s60, 0\n"
    "s_mov_b32 s62, 8\n"
    "s_getpc_b64 s[94:95]\n"
    "s_add_u32 s94, s94, 12\n"
    "s_addc_u32 s95, s95, 0\n"
    "s_branch L_FB%=\n"
    "v_accvgpr_read_b32 v0, a168\n"
    "v_accvgpr_read_b32 v1, a169\n"
    "v_accvgpr_read_b32 v2, a170\n"
    "v_accvgpr_read_b32 v3, a171\n"
    "v_accvgpr_read_b32 v4, a172\n"
    "v_accvgpr_read_b32 v5, a173\n"
    "v_accvgpr_read_b32 v6, a174\n"
    "v_accvgpr_read_b32 v7, a175\n"
    "v_accvgpr_read_b32 v8, a176\n"
    "v_accvgpr_read_b32 v9, a177\n"
    "v_accvgpr_read_b32 v10, a178\n"
    "v_accvgpr_read_b32 v11, a179\n"
    "v_accvgpr_read_b32 v12, a180\n"
    "v_accvgpr_read_b32 v13, a181\n"
    "v_accvgpr_read_b32 v14, a182\n"
    "v_accvgpr_read_b32 v15, a183\n"
    "v_rcp_f32 v224, v8\n"
    "s_nop 1\n"
    "s_branch L_norm10%=\n"
    "L_fb11%=:\n"
    "v_accvgpr_write_b32 a152, v204\n"
    "v_accvgpr_write_b32 a153, v205\n"
    "v_accvgpr_write_b32 a154, v206\n"
    "v_accvgpr_write_b32 a155, v207\n"
    "s_lshl_b32 s33, s28, 1\n"
    "s_add_u32 s33, s33, 1\n"
    "s_mov_b64 s[96:97], s[68:69]\n"
    "s_mov_b32 s98, 256\n"
    "s_mov_b32 s99, 0\n"
    "s_mov_b32 s60, 0\n"
    "s_mov_b32 s62, 8\n"
    "s_getpc_b64 s[94:95]\n"
    "s_add_u32 s94, s94, 12\n"
    "s_addc_u32 s95, s95, 0\n"
    "s_branch L_FB%=\n"
    "v_accvgpr_read_b32 v16, a168\n"
    "v_accvgpr_read_b32 v17, a169\n"
    "v_accvgpr_read_b32 v18, a170\n"
    "v_accvgpr_read_b32 v19, a171\n"
    "v_accvgpr_read_b32 v20, a172\n"
    "v_accvgpr_read_b32 v21, a173\n"
    "v_accvgpr_read_b32 v22, a174\n"
    "v_accvgpr_read_b32 v23, a175\n"
    "v_accvgpr_read_b32 v24, a176\n"
    "v_accvgpr_read_b32 v25, a177\n"
    "v_accvgpr_read_b32 v26, a178\n"
    "v_accvgpr_read_b32 v27, a179\n"
    "v_accvgpr_read_b32 v28, a180\n"
    "v_accvgpr_read_b32 v29, a181\n"
    "v_accvgpr_read_b32 v30, a182\n"
    "v_accvgpr_read_b32 v31, a183\n"
    "v_rcp_f32 v224, v24\n"
    "s_nop 1\n"
    "s_branch L_norm11%=\n"
    "L_reA0%=:\n"
    "s_add_u32 s98, s18, 0\n"
    "buffer_load_dwordx4 v[128:131], v237, s[88:91], s98 offen sc1\n"
    "buffer_load_dwordx4 v[132:135], v237, s[88:91], s98 offen offset:1024 sc1\n"
    "s_add_u32 s98, s18, 65536\n"
    "buffer_load_dwordx4 v[160:163], v237, s[88:91], s98 offen sc1\n"
    "buffer_load_dwordx4 v[164:167], v237, s[88:91], s98 offen offset:1024 sc1\n"
    "s_add_u32 s98, s19, 0\n"
    "buffer_load_dwordx4 v[136:139], v237, s[88:91], s98 offen sc1\n"
    "buffer_load_dwordx4 v[140:143], v237, s[88:91], s98 offen offset:1024 sc1\n"
    "s_add_u32 s98, s19, 65536\n"
    "buffer_load_dwordx4 v[168:171], v237, s[88:91], s98 offen sc1\n"
    "buffer_load_dwordx4 v[172:175], v237, s[88:91], s98 offen offset:1024 sc1\n"
    "s_add_u32 s98, s22, 0\n"
    "buffer_load_dwordx4 v[144:147], v237, s[88:91], s98 offen sc1\n"
    "buffer_load_dwordx4 v[148:151], v237, s[88:91], s98 offen offset:1024 sc1\n"
    "s_add_u32 s98, s22, 65536\n"
    "buffer_load_dwordx4 v[176:179], v237, s[88:91], s98 offen sc1\n"
    "buffer_load_dwordx4 v[180:183], v237, s[88:91], s98 offen offset:1024 sc1\n"
    "s_add_u32 s98, s23, 0\n"
    "buffer_load_dwordx4 v[152:155], v237, s[88:91], s98 offen sc1\n"
    "buffer_load_dwordx4 v[156:159], v237, s[88:91], s98 offen offset:1024 sc1\n"
    "s_add_u32 s98, s23, 65536\n"
    "buffer_load_dwordx4 v[184:187], v237, s[88:91], s98 offen sc1\n"
    "buffer_load_dwordx4 v[188:191], v237, s[88:91], s98 offen offset:1024 sc1\n"
    "s_add_u32 s92, s92, 1\n"
    "s_cmp_gt_u32 s92, 0x40000\n"
    "s_waitcnt vmcnt(0)\n"
    "s_cbranch_scc1 L_goA0%=\n"
    "s_branch L_ckA0%=\n"
    "L_reA1%=:\n"
    "s_add_u32 s98, s18, 0\n"
    "buffer_load_dwordx4 v[128:131], v237, s[88:91], s98 offen sc1\n"
    "buffer_load_dwordx4 v[132:135], v237, s[88:91], s98 offen offset:1024 sc1\n"
    "s_add_u32 s98, s18, 65536\n"
    "buffer_load_dwordx4 v[160:163], v237, s[88:91], s98 offen sc1\n"
    "buffer_load_dwordx4 v[164:167], v237, s[88:91], s98 offen offset:1024 sc1\n"
    "s_add_u32 s98, s19, 0\n"
    "buffer_load_dwordx4 v[136:139], v237, s[88:91], s98 offen sc1\n"
    "buffer_load_dwordx4 v[140:143], v237, s[88:91], s98 offen offset:1024 sc1\n"
    "s_add_u32 s98, s19, 65536\n"
    "buffer_load_dwordx4 v[168:171], v237, s[88:91], s98 offen sc1\n"
    "buffer_load_dwordx4 v[172:175], v237, s[88:91], s98 offen offset:1024 sc1\n"
    "s_add_u32 s98, s22, 0\n"
    "buffer_load_dwordx4 v[144:147], v237, s[88:91], s98 offen sc1\n"
    "buffer_load_dwordx4 v[148:151], v237, s[88:91], s98 offen offset:1024 sc1\n"
    "s_add_u32 s98, s22, 65536\n"
    "buffer_load_dwordx4 v[176:179], v237, s[88:91], s98 offen sc1\n"
    "buffer_load_dwordx4 v[180:183], v237, s[88:91], s98 offen offset:1024 sc1\n"
    "s_add_u32 s98, s23, 0\n"
    "buffer_load_dwordx4 v[152:155], v237, s[88:91], s98 offen sc1\n"
    "buffer_load_dwordx4 v[156:159], v237, s[88:91], s98 offen offset:1024 sc1\n"
    "s_add_u32 s98, s23, 65536\n"
    "buffer_load_dwordx4 v[184:187], v237, s[88:91], s98 offen sc1\n"
    "buffer_load_dwordx4 v[188:191], v237, s[88:91], s98 offen offset:1024 sc1\n"
    "s_add_u32 s92, s92, 1\n"
    "s_cmp_gt_u32 s92, 0x40000\n"
    "s_waitcnt vmcnt(0)\n"
    "s_cbranch_scc1 L_goA1%=\n"
    "s_branch L_ckA1%=\n"
    "L_reA2%=:\n"
    "s_add_u32 s98, s19, 0\n"
    "buffer_load_dwordx4 v[136:139], v237, s[88:91], s98 offen sc1\n"
    "buffer_load_dwordx4 v[140:143], v237, s[88:91], s98 offen offset:1024 sc1\n"
    "s_add_u32 s98, s19, 65536\n"
    "buffer_load_dwordx4 v[168:171], v237, s[88:91], s98 offen sc1\n"
    "buffer_load_dwordx4 v[172:175], v237, s[88:91], s98 offen offset:1024 sc1\n"
    "s_add_u32 s98, s22, 0\n"
    "buffer_load_dwordx4 v[144:147], v237, s[88:91], s98 offen sc1\n"
    "buffer_load_dwordx4 v[148:151], v237, s[88:91], s98 offen offset:1024 sc1\n"
    "s_add_u32 s98, s22, 65536\n"
    "buffer_load_dwordx4 v[176:179], v237, s[88:91], s98 offen sc1\n"
    "buffer_load_dwordx4 v[180:183], v237, s[88:91], s98 offen offset:1024 sc1\n"
    "s_add_u32 s98, s23, 0\n"
    "buffer_load_dwordx4 v[152:155], v237, s[88:91], s98 offen sc1\n"
    "buffer_load_dwordx4 v[156:159], v237, s[88:91], s98 offen offset:1024 sc1\n"
    "s_add_u32 s98, s23, 65536\n"
    "buffer_load_dwordx4 v[184:187], v237, s[88:91], s98 offen sc1\n"
    "buffer_load_dwordx4 v[188:191], v237, s[88:91], s98 offen offset:1024 sc1\n"
    "s_add_u32 s92, s92, 1\n"
    "s_cmp_gt_u32 s92, 0x40000\n"
    "s_waitcnt vmcnt(0)\n"
    "s_cbranch_scc1 L_goA2%=\n"
    "s_branch L_ckA2%=\n"
    "L_reA3%=:\n"
    "s_add_u32 s98, s19, 0\n"
    "buffer_load_dwordx4 v[136:139], v237, s[88:91], s98 offen sc1\n"
    "buffer_load_dwordx4 v[140:143], v237, s[88:91], s98 offen offset:1024 sc1\n"
    "s_add_u32 s98, s19, 65536\n"
    "buffer_load_dwordx4 v[168:171], v237, s[88:91], s98 offen sc1\n"
    "buffer_load_dwordx4 v[172:175], v237, s[88:91], s98 offen offset:1024 sc1\n"
    "s_add_u32 s98, s22, 0\n"
    "buffer_load_dwordx4 v[144:147], v237, s[88:91], s98 offen sc1\n"
    "buffer_load_dwordx4 v[148:151], v237, s[88:91], s98 offen offset:1024 sc1\n"
    "s_add_u32 s98, s22, 65536\n"
    "buffer_load_dwordx4 v[176:179], v237, s[88:91], s98 offen sc1\n"
    "buffer_load_dwordx4 v[180:183], v237, s[88:91], s98 offen offset:1024 sc1\n"
    "s_add_u32 s98, s23, 0\n"
    "buffer_load_dwordx4 v[152:155], v237, s[88:91], s98 offen sc1\n"
    "buffer_load_dwordx4 v[156:159], v237, s[88:91], s98 offen offset:1024 sc1\n"
    "s_add_u32 s98, s23, 65536\n"
    "buffer_load_dwordx4 v[184:187], v237, s[88:91], s98 offen sc1\n"
    "buffer_load_dwordx4 v[188:191], v237, s[88:91], s98 offen offset:1024 sc1\n"
    "s_add_u32 s92, s92, 1\n"
    "s_cmp_gt_u32 s92, 0x40000\n"
    "s_waitcnt vmcnt(0)\n"
    "s_cbranch_scc1 L_goA3%=\n"
    "s_branch L_ckA3%=\n"
    "L_reA4%=:\n"
    "s_add_u32 s98, s22, 0\n"
    "buffer_load_dwordx4 v[144:147], v237, s[88:91], s98 offen sc1\n"
    "buffer_load_dwordx4 v[148:151], v237, s[88:91], s98 offen offset:1024 sc1\n"
    "s_add_u32 s98, s22, 65536\n"
    "buffer_load_dwordx4 v[176:179], v237, s[88:91], s98 offen sc1\n"
    "buffer_load_dwordx4 v[180:183], v237, s[88:91], s98 offen offset:1024 sc1\n"
    "s_add_u32 s98, s23, 0\n"
    "buffer_load_dwordx4 v[152:155], v237, s[88:91], s98 offen sc1\n"
    "buffer_load_dwordx4 v[156:159], v237, s[88:91], s98 offen offset:1024 sc1\n"
    "s_add_u32 s98, s23, 65536\n"
    "buffer_load_dwordx4 v[184:187], v237, s[88:91], s98 offen sc1\n"
    "buffer_load_dwordx4 v[188:191], v237, s[88:91], s98 offen offset:1024 sc1\n"
    "s_add_u32 s92, s92, 1\n"
    "s_cmp_gt_u32 s92, 0x40000\n"
    "s_waitcnt vmcnt(0)\n"
    "s_cbranch_scc1 L_goA4%=\n"
    "s_branch L_ckA4%=\n"
    "L_reA5%=:\n"
    "s_add_u32 s98, s22, 0\n"
    "buffer_load_dwordx4 v[144:147], v237, s[88:91], s98 offen sc1\n"
    "buffer_load_dwordx4 v[148:151], v237, s[88:91], s98 offen offset:1024 sc1\n"
    "s_add_u32 s98, s22, 65536\n"
    "buffer_load_dwordx4 v[176:179], v237, s[88:91], s98 offen sc1\n"
    "buffer_load_dwordx4 v[180:183], v237, s[88:91], s98 offen offset:1024 sc1\n"
    "s_add_u32 s98, s23, 0\n"
    "buffer_load_dwordx4 v[152:155], v237, s[88:91], s98 offen sc1\n"
    "buffer_load_dwordx4 v[156:159], v237, s[88:91], s98 offen offset:1024 sc1\n"
    "s_add_u32 s98, s23, 65536\n"
    "buffer_load_dwordx4 v[184:187], v237, s[88:91], s98 offen sc1\n"
    "buffer_load_dwordx4 v[188:191], v237, s[88:91], s98 offen offset:1024 sc1\n"
    "s_add_u32 s92, s92, 1\n"
    "s_cmp_gt_u32 s92, 0x40000\n"
    "s_waitcnt vmcnt(0)\n"
    "s_cbranch_scc1 L_goA5%=\n"
    "s_branch L_ckA5%=\n"
    "L_reA6%=:\n"
    "s_add_u32 s98, s23, 0\n"
    "buffer_load_dwordx4 v[152:155], v237, s[88:91], s98 offen sc1\n"
    "buffer_load_dwordx4 v[156:159], v237, s[88:91], s98 offen offset:1024 sc1\n"
    "s_add_u32 s98, s23, 65536\n"
    "buffer_load_dwordx4 v[184:187], v237, s[88:91], s98 offen sc1\n"
    "buffer_load_dwordx4 v[188:191], v237, s[88:91], s98 offen offset:1024 sc1\n"
    "s_add_u32 s92, s92, 1\n"
    "s_cmp_gt_u32 s92, 0x40000\n"
    "s_waitcnt vmcnt(0)\n"
    "s_cbranch_scc1 L_goA6%=\n"
    "s_branch L_ckA6%=\n"
    "L_reA7%=:\n"
    "s_add_u32 s98, s23, 0\n"
    "buffer_load_dwordx4 v[152:155], v237, s[88:91], s98 offen sc1\n"
    "buffer_load_dwordx4 v[156:159], v237, s[88:91], s98 offen offset:1024 sc1\n"
    "s_add_u32 s98, s23, 65536\n"
    "buffer_load_dwordx4 v[184:187], v237, s[88:91], s98 offen sc1\n"
    "buffer_load_dwordx4 v[188:191], v237, s[88:91], s98 offen offset:1024 sc1\n"
    "s_add_u32 s92, s92, 1\n"
    "s_cmp_gt_u32 s92, 0x40000\n"
    "s_waitcnt vmcnt(0)\n"
    "s_cbranch_scc1 L_goA7%=\n"
    "s_branch L_ckA7%=\n"
    "L_reB00%=:\n"
    "s_add_u32 s98, s18, 32768\n"
    "buffer_load_dwordx4 v[32:35], v237, s[88:91], s98 offen sc1\n"
    "buffer_load_dwordx4 v[36:39], v237, s[88:91], s98 offen offset:1024 sc1\n"
    "s_add_u32 s98, s19, 32768\n"
    "buffer_load_dwordx4 v[40:43], v237, s[88:91], s98 offen sc1\n"
    "buffer_load_dwordx4 v[44:47], v237, s[88:91], s98 offen offset:1024 sc1\n"
    "s_add_u32 s98, s22, 32768\n"
    "buffer_load_dwordx4 v[48:51], v237, s[88:91], s98 offen sc1\n"
    "buffer_load_dwordx4 v[52:55], v237, s[88:91], s98 offen offset:1024 sc1\n"
    "s_add_u32 s98, s23, 32768\n"
    "buffer_load_dwordx4 v[56:59], v237, s[88:91], s98 offen sc1\n"
    "buffer_load_dwordx4 v[60:63], v237, s[88:91], s98 offen offset:1024 sc1\n"
    "s_add_u32 s92, s92, 1\n"
    "s_cmp_gt_u32 s92, 0x40000\n"
    "s_waitcnt vmcnt(0)\n"
    "s_cbranch_scc1 L_goB00%=\n"
    "s_branch L_ckB00%=\n"
    "L_reB01%=:\n"
    "s_add_u32 s98, s19, 32768\n"
    "buffer_load_dwordx4 v[40:43], v237, s[88:91], s98 offen sc1\n"
    "buffer_load_dwordx4 v[44:47], v237, s[88:91], s98 offen offset:1024 sc1\n"
    "s_add_u32 s98, s22, 32768\n"
    "buffer_load_dwordx4 v[48:51], v237, s[88:91], s98 offen sc1\n"
    "buffer_load_dwordx4 v[52:55], v237, s[88:91], s98 offen offset:1024 sc1\n"
    "s_add_u32 s98, s23, 32768\n"
    "buffer_load_dwordx4 v[56:59], v237, s[88:91], s98 offen sc1\n"
    "buffer_load_dwordx4 v[60:63], v237, s[88:91], s98 offen offset:1024 sc1\n"
    "s_add_u32 s92, s92, 1\n"
    "s_cmp_gt_u32 s92, 0x40000\n"
    "s_waitcnt vmcnt(0)\n"
    "s_cbranch_scc1 L_goB01%=\n"
    "s_branch L_ckB01%=\n"
    "L_reB02%=:\n"
    "s_add_u32 s98, s22, 32768\n"
    "buffer_load_dwordx4 v[48:51], v237, s[88:91], s98 offen sc1\n"
    "buffer_load_dwordx4 v[52:55], v237, s[88:91], s98 offen offset:1024 sc1\n"
    "s_add_u32 s98, s23, 32768\n"
    "buffer_load_dwordx4 v[56:59], v237, s[88:91], s98 offen sc1\n"
    "buffer_load_dwordx4 v[60:63], v237, s[88:91], s98 offen offset:1024 sc1\n"
    "s_add_u32 s92, s92, 1\n"
    "s_cmp_gt_u32 s92, 0x40000\n"
    "s_waitcnt vmcnt(0)\n"
    "s_cbranch_scc1 L_goB02%=\n"
    "s_branch L_ckB02%=\n"
    "L_reB03%=:\n"
    "s_add_u32 s98, s23, 32768\n"
    "buffer_load_dwordx4 v[56:59], v237, s[88:91], s98 offen sc1\n"
    "buffer_load_dwordx4 v[60:63], v237, s[88:91], s98 offen offset:1024 sc1\n"
    "s_add_u32 s92, s92, 1\n"
    "s_cmp_gt_u32 s92, 0x40000\n"
    "s_waitcnt vmcnt(0)\n"
    "s_cbranch_scc1 L_goB03%=\n"
    "s_branch L_ckB03%=\n"
    "L_reB10%=:\n"
    "s_add_u32 s98, s18, 98304\n"
    "buffer_load_dwordx4 v[96:99], v237, s[88:91], s98 offen sc1\n"
    "buffer_load_dwordx4 v[100:103], v237, s[88:91], s98 offen offset:1024 sc1\n"
    "s_add_u32 s98, s19, 98304\n"
    "buffer_load_dwordx4 v[104:107], v237, s[88:91], s98 offen sc1\n"
    "buffer_load_dwordx4 v[108:111], v237, s[88:91], s98 offen offset:1024 sc1\n"
    "s_add_u32 s98, s22, 98304\n"
    "buffer_load_dwordx4 v[112:115], v237, s[88:91], s98 offen sc1\n"
    "buffer_load_dwordx4 v[116:119], v237, s[88:91], s98 offen offset:1024 sc1\n"
    "s_add_u32 s98, s23, 98304\n"
    "buffer_load_dwordx4 v[120:123], v237, s[88:91], s98 offen sc1\n"
    "buffer_load_dwordx4 v[124:127], v237, s[88:91], s98 offen offset:1024 sc1\n"
    "s_add_u32 s92, s92, 1\n"
    "s_cmp_gt_u32 s92, 0x40000\n"
    "s_waitcnt vmcnt(0)\n"
    "s_cbranch_scc1 L_goB10%=\n"
    "s_branch L_ckB10%=\n"
    "L_reB11%=:\n"
    "s_add_u32 s98, s19, 98304\n"
    "buffer_load_dwordx4 v[104:107], v237, s[88:91], s98 offen sc1\n"
    "buffer_load_dwordx4 v[108:111], v237, s[88:91], s98 offen offset:1024 sc1\n"
    "s_add_u32 s98, s22, 98304\n"
    "buffer_load_dwordx4 v[112:115], v237, s[88:91], s98 offen sc1\n"
    "buffer_load_dwordx4 v[116:119], v237, s[88:91], s98 offen offset:1024 sc1\n"
    "s_add_u32 s98, s23, 98304\n"
    "buffer_load_dwordx4 v[120:123], v237, s[88:91], s98 offen sc1\n"
    "buffer_load_dwordx4 v[124:127], v237, s[88:91], s98 offen offset:1024 sc1\n"
    "s_add_u32 s92, s92, 1\n"
    "s_cmp_gt_u32 s92, 0x40000\n"
    "s_waitcnt vmcnt(0)\n"
    "s_cbranch_scc1 L_goB11%=\n"
    "s_branch L_ckB11%=\n"
    "L_reB12%=:\n"
    "s_add_u32 s98, s22, 98304\n"
    "buffer_load_dwordx4 v[112:115], v237, s[88:91], s98 offen sc1\n"
    "buffer_load_dwordx4 v[116:119], v237, s[88:91], s98 offen offset:1024 sc1\n"
    "s_add_u32 s98, s23, 98304\n"
    "buffer_load_dwordx4 v[120:123], v237, s[88:91], s98 offen sc1\n"
    "buffer_load_dwordx4 v[124:127], v237, s[88:91], s98 offen offset:1024 sc1\n"
    "s_add_u32 s92, s92, 1\n"
    "s_cmp_gt_u32 s92, 0x40000\n"
    "s_waitcnt vmcnt(0)\n"
    "s_cbranch_scc1 L_goB12%=\n"
    "s_branch L_ckB12%=\n"
    "L_reB13%=:\n"
    "s_add_u32 s98, s23, 98304\n"
    "buffer_load_dwordx4 v[120:123], v237, s[88:91], s98 offen sc1\n"
    "buffer_load_dwordx4 v[124:127], v237, s[88:91], s98 offen offset:1024 sc1\n"
    "s_add_u32 s92, s92, 1\n"
    "s_cmp_gt_u32 s92, 0x40000\n"
    "s_waitcnt vmcnt(0)\n"
    "s_cbranch_scc1 L_goB13%=\n"
    "s_branch L_ckB13%=\n"
    "L_FB%=:\n"
    "s_nop 15\n"
    "s_nop 15\n"
    "v_accvgpr_write_b32 a216, v64\n"
    "v_accvgpr_write_b32 a217, v65\n"
    "v_accvgpr_write_b32 a218, v66\n"
    "v_accvgpr_write_b32 a219, v67\n"
    "v_accvgpr_write_b32 a220, v68\n"
    "v_accvgpr_write_b32 a221, v69\n"
    "v_accvgpr_write_b32 a222, v70\n"
    "v_accvgpr_write_b32 a223, v71\n"
    "v_accvgpr_write_b32 a224, v72\n"
    "v_accvgpr_write_b32 a225, v73\n"
    "v_accvgpr_write_b32 a226, v74\n"
    "v_accvgpr_write_b32 a227, v75\n"
    "v_accvgpr_write_b32 a228, v76\n"
    "v_accvgpr_write_b32 a229, v77\n"
    "v_accvgpr_write_b32 a230, v78\n"
    "v_accvgpr_write_b32 a231, v79\n"
    "v_accvgpr_write_b32 a232, v216\n"
    "v_accvgpr_write_b32 a233, v217\n"
    "v_accvgpr_write_b32 a234, v218\n"
    "v_accvgpr_write_b32 a235, v219\n"
    "v_accvgpr_write_b32 a236, v220\n"
    "v_accvgpr_write_b32 a237, v221\n"
    "v_accvgpr_write_b32 a238, v222\n"
    "v_accvgpr_write_b32 a239, v223\n"
    "v_accvgpr_write_b32 a240, s35\n"
    "v_accvgpr_write_b32 a160, v239\n"
    "v_accvgpr_write_b32 a161, v239\n"
    "v_accvgpr_write_b32 a162, v239\n"
    "v_accvgpr_write_b32 a163, v239\n"
    "v_accvgpr_write_b32 a164, v239\n"
    "v_accvgpr_write_b32 a165, v239\n"
    "v_accvgpr_write_b32 a166, v239\n"
    "v_accvgpr_write_b32 a167, v239\n"
    "v_accvgpr_write_b32 a168, 0\n"
    "v_accvgpr_write_b32 a169, 0\n"
    "v_accvgpr_write_b32 a170, 0\n"
    "v_accvgpr_write_b32 a171, 0\n"
    "v_accvgpr_write_b32 a172, 0\n"
    "v_accvgpr_write_b32 a173, 0\n"
    "v_accvgpr_write_b32 a174, 0\n"
    "v_accvgpr_write_b32 a175, 0\n"
    "v_accvgpr_write_b32 a176, 0\n"
    "v_accvgpr_write_b32 a177, 0\n"
    "v_accvgpr_write_b32 a178, 0\n"
    "v_accvgpr_write_b32 a179, 0\n"
    "v_accvgpr_write_b32 a180, 0\n"
    "v_accvgpr_write_b32 a181, 0\n"
    "v_accvgpr_write_b32 a182, 0\n"
    "v_accvgpr_write_b32 a183, 0\n"
    "v_mov_b32 v228, v243\n"
    "s_sub_i32 s56, s33, s62\n"
    "s_max_i32 s56, s56, 0\n"
    "s_mov_b32 s61, 0\n"
    "L_FBp%=:\n"
    "s_mov_b32 s34, s56\n"
    "L_FBl%=:\n"
    "s_lshr_b32 s35, s34, 1\n"
    "s_lshl_b32 s63, s57, 5\n"
    "s_add_u32 s35, s35, s63\n"
    "s_lshl_b32 s35, s35, 13\n"
    "s_and_b32 s63, s34, 1\n"
    "s_lshl_b32 s63, s63, 10\n"
    "s_add_u32 s35, s35, s63\n"
    "s_add_u32 s63, s35, s63\n"
    "s_add_u32 s35, s35, s60\n"
    "buffer_load_dwordx4 a[156:159], v237, s[52:55], s35 offen sc1\n"
    "s_mov_b64 exec, s[96:97]\n"
    "buffer_load_dwordx4 a[164:167], v238, s[52:55], s63 offen offset:1024 sc1\n"
    "s_sub_u32 s35, s33, s34\n"
    "s_and_b32 s93, s35, s99\n"
    "s_cselect_b32 s63, 0x7ff00000, s63\n"
    "s_mov_b32 s93, 0x1f8\n"
    "buffer_load_dwordx4 a[160:163], v238, s[52:55], s63 offen sc1\n"
    "s_mov_b64 exec, -1\n"
    "s_lshl_b32 s35, s35, 5\n"
    "v_add_u32 v229, s35, v240\n"
    "s_waitcnt vmcnt(0)\n"
    "v_mfma_f32_32x32x16_bf16 v[64:79], a[156:159], a[152:155], 0\n"
    "v_subrev_u32 v234, 0, v229\n"
    "v_and_b32 v235, s99, v234\n"
    "v_lshl_or_b32 v234, v235, 31, v234\n"
    "v_cmp_gt_u32 vcc, s98, v234\n"
    "s_nop 7\n"
    "v_cndmask_b32 v64, v243, v64, vcc\n"
    "v_subrev_u32 v234, 1, v229\n"
    "v_and_b32 v235, s99, v234\n"
    "v_lshl_or_b32 v234, v235, 31, v234\n"
    "v_cmp_gt_u32 vcc, s98, v234\n"
    "s_nop 1\n"
    "v_cndmask_b32 v65, v243, v65, vcc\n"
    "v_subrev_u32 v234, 2, v229\n"
    "v_and_b32 v235, s99, v234\n"
    "v_lshl_or_b32 v234, v235, 31, v234\n"
    "v_cmp_gt_u32 vcc, s98, v234\n"
    "s_nop 1\n"
    "v_cndmask_b32 v66, v243, v66, vcc\n"
    "v_subrev_u32 v234, 3, v229\n"
    "v_and_b32 v235, s99, v234\n"
    "v_lshl_or_b32 v234, v235, 31, v234\n"
    "v_cmp_gt_u32 vcc, s98, v234\n"
    "s_nop 1\n"
    "v_cndmask_b32 v67, v243, v67, vcc\n"
    "v_subrev_u32 v234, 8, v229\n"
    "v_and_b32 v235, s99, v234\n"
    "v_lshl_or_b32 v234, v235, 31, v234\n"
    "v_cmp_gt_u32 vcc, s98, v234\n"
    "s_nop 1\n"
    "v_cndmask_b32 v68, v243, v68, vcc\n"
    "v_subrev_u32 v234, 9, v229\n"
    "v_and_b32 v235, s99, v234\n"
    "v_lshl_or_b32 v234, v235, 31, v234\n"
    "v_cmp_gt_u32 vcc, s98, v234\n"
    "s_nop 1\n"
    "v_cndmask_b32 v69, v243, v69, vcc\n"
    "v_subrev_u32 v234, 10, v229\n"
    "v_and_b32 v235, s99, v234\n"
    "v_lshl_or_b32 v234, v235, 31, v234\n"
    "v_cmp_gt_u32 vcc, s98, v234\n"
    "s_nop 1\n"
    "v_cndmask_b32 v70, v243, v70, vcc\n"
    "v_subrev_u32 v234, 11, v229\n"
    "v_and_b32 v235, s99, v234\n"
    "v_lshl_or_b32 v234, v235, 31, v234\n"
    "v_cmp_gt_u32 vcc, s98, v234\n"
    "s_nop 1\n"
    "v_cndmask_b32 v71, v243, v71, vcc\n"
    "v_subrev_u32 v234, 16, v229\n"
    "v_and_b32 v235, s99, v234\n"
    "v_lshl_or_b32 v234, v235, 31, v234\n"
    "v_cmp_gt_u32 vcc, s98, v234\n"
    "s_nop 1\n"
    "v_cndmask_b32 v72, v243, v72, vcc\n"
    "v_subrev_u32 v234, 17, v229\n"
    "v_and_b32 v235, s99, v234\n"
    "v_lshl_or_b32 v234, v235, 31, v234\n"
    "v_cmp_gt_u32 vcc, s98, v234\n"
    "s_nop 1\n"
    "v_cndmask_b32 v73, v243, v73, vcc\n"
    "v_subrev_u32 v234, 18, v229\n"
    "v_and_b32 v235, s99, v234\n"
    "v_lshl_or_b32 v234, v235, 31, v234\n"
    "v_cmp_gt_u32 vcc, s98, v234\n"
    "s_nop 1\n"
    "v_cndmask_b32 v74, v243, v74, vcc\n"
    "v_subrev_u32 v234, 19, v229\n"
    "v_and_b32 v235, s99, v234\n"
    "v_lshl_or_b32 v234, v235, 31, v234\n"
    "v_cmp_gt_u32 vcc, s98, v234\n"
    "s_nop 1\n"
    "v_cndmask_b32 v75, v243, v75, vcc\n"
    "v_subrev_u32 v234, 24, v229\n"
    "v_and_b32 v235, s99, v234\n"
    "v_lshl_or_b32 v234, v235, 31, v234\n"
    "v_cmp_gt_u32 vcc, s98, v234\n"
    "s_nop 1\n"
    "v_cndmask_b32 v76, v243, v76, vcc\n"
    "v_subrev_u32 v234, 25, v229\n"
    "v_and_b32 v235, s99, v234\n"
    "v_lshl_or_b32 v234, v235, 31, v234\n"
    "v_cmp_gt_u32 vcc, s98, v234\n"
    "s_nop 1\n"
    "v_cndmask_b32 v77, v243, v77, vcc\n"
    "v_subrev_u32 v234, 26, v229\n"
    "v_and_b32 v235, s99, v234\n"
    "v_lshl_or_b32 v234, v235, 31, v234\n"
    "v_cmp_gt_u32 vcc, s98, v234\n"
    "s_nop 1\n"
    "v_cndmask_b32 v78, v243, v78, vcc\n"
    "v_subrev_u32 v234, 27, v229\n"
    "v_and_b32 v235, s99, v234\n"
    "v_lshl_or_b32 v234, v235, 31, v234\n"
    "v_cmp_gt_u32 vcc, s98, v234\n"
    "s_nop 1\n"
    "v_cndmask_b32 v79, v243, v79, vcc\n"
    "s_cmp_eq_u32 s61, 0\n"
    "s_cbranch_scc0 L_FBe%=\n"
    "v_max3_f32 v228, v228, v64, v65\n"
    "v_max3_f32 v228, v228, v66, v67\n"
    "v_max3_f32 v228, v228, v68, v69\n"
    "v_max3_f32 v228, v228, v70, v71\n"
    "v_max3_f32 v228, v228, v72, v73\n"
    "v_max3_f32 v228, v228, v74, v75\n"
    "v_max3_f32 v228, v228, v76, v77\n"
    "v_max3_f32 v228, v228, v78, v79\n"
    "s_branch L_FBn%=\n"
    "L_FBe%=:\n"
    "v_sub_f32 v64, v64, v228\n"
    "v_sub_f32 v65, v65, v228\n"
    "v_sub_f32 v66, v66, v228\n"
    "v_sub_f32 v67, v67, v228\n"
    "v_sub_f32 v68, v68, v228\n"
    "v_sub_f32 v69, v69, v228\n"
    "v_sub_f32 v70, v70, v228\n"
    "v_sub_f32 v71, v71, v228\n"
    "v_sub_f32 v72, v72, v228\n"
    "v_sub_f32 v73, v73, v228\n"
    "v_sub_f32 v74, v74, v228\n"
    "v_sub_f32 v75, v75, v228\n"
    "v_sub_f32 v76, v76, v228\n"
    "v_sub_f32 v77, v77, v228\n"
    "v_sub_f32 v78, v78, v228\n"
    "v_sub_f32 v79, v79, v228\n"
    "v_exp_f32 v64, v64\n"
    "v_exp_f32 v65, v65\n"
    "v_exp_f32 v66, v66\n"
    "v_exp_f32 v67, v67\n"
    "v_exp_f32 v68, v68\n"
    "v_exp_f32 v69, v69\n"
    "v_exp_f32 v70, v70\n"
    "v_exp_f32 v71, v71\n"
    "v_exp_f32 v72, v72\n"
    "v_exp_f32 v73, v73\n"
    "v_exp_f32 v74, v74\n"
    "v_exp_f32 v75, v75\n"
    "v_exp_f32 v76, v76\n"
    "v_exp_f32 v77, v77\n"
    "v_exp_f32 v78, v78\n"
    "v_exp_f32 v79, v79\n"
    "v_cvt_pk_bf16_f32 v216, v64, v65\n"
    "v_cvt_pk_bf16_f32 v217, v66, v67\n"
    "v_cvt_pk_bf16_f32 v218, v68, v69\n"
    "v_cvt_pk_bf16_f32 v219, v70, v71\n"
    "v_cvt_pk_bf16_f32 v220, v72, v73\n"
    "v_cvt_pk_bf16_f32 v221, v74, v75\n"
    "v_cvt_pk_bf16_f32 v222, v76, v77\n"
    "v_cvt_pk_bf16_f32 v223, v78, v79\n"
    "v_mfma_f32_32x32x16_bf16 a[168:183], a[160:163], v[216:219], a[168:183]\n"
    "s_nop 0\n"
    "v_mfma_f32_32x32x16_bf16 a[168:183], a[164:167], v[220:223], a[168:183]\n"
    "s_nop 15\n"
    "L_FBn%=:\n"
    "s_add_u32 s34, s34, 1\n"
    "s_cmp_le_u32 s34, s33\n"
    "s_cbranch_scc1 L_FBl%=\n"
    "s_cmp_eq_u32 s61, 0\n"
    "s_cbranch_scc0 L_FBd%=\n"
    "v_mov_b32 v229, v228\n"
    "s_mov_b32 s61, 1\n"
    "s_nop 1\n"
    "v_permlane32_swap_b32 v229, v228\n"
    "s_nop 1\n"
    "v_max_f32 v228, v228, v229\n"
    "s_branch L_FBp%=\n"
    "L_FBd%=:\n"
    "s_nop 15\n"
    "v_accvgpr_read_b32 v64, a216\n"
    "v_accvgpr_read_b32 v65, a217\n"
    "v_accvgpr_read_b32 v66, a218\n"
    "v_accvgpr_read_b32 v67, a219\n"
    "v_accvgpr_read_b32 v68, a220\n"
    "v_accvgpr_read_b32 v69, a221\n"
    "v_accvgpr_read_b32 v70, a222\n"
    "v_accvgpr_read_b32 v71, a223\n"
    "v_accvgpr_read_b32 v72, a224\n"
    "v_accvgpr_read_b32 v73, a225\n"
    "v_accvgpr_read_b32 v74, a226\n"
    "v_accvgpr_read_b32 v75, a227\n"
    "v_accvgpr_read_b32 v76, a228\n"
    "v_accvgpr_read_b32 v77, a229\n"
    "v_accvgpr_read_b32 v78, a230\n"
    "v_accvgpr_read_b32 v79, a231\n"
    "v_accvgpr_read_b32 v216, a232\n"
    "v_accvgpr_read_b32 v217, a233\n"
    "v_accvgpr_read_b32 v218, a234\n"
    "v_accvgpr_read_b32 v219, a235\n"
    "v_accvgpr_read_b32 v220, a236\n"
    "v_accvgpr_read_b32 v221, a237\n"
    "v_accvgpr_read_b32 v222, a238\n"
    "v_accvgpr_read_b32 v223, a239\n"
    "v_accvgpr_read_b32 v229, a240\n"
    "s_nop 0\n"
    "v_readfirstlane_b32 s35, v229\n"
    "s_nop 4\n"
    "s_setpc_b64 s[94:95]\n"
    :
    : "s"(__builtin_amdgcn_kernarg_segment_ptr()), "s"(blockIdx.x), "s"(wave), "s"((unsigned)(size_t)lds)
    :
      "v0", "v1", "v2", "v3", "v4", "v5", "v6", "v7", "v8", "v9", "v10", "v11", "v12", "v13", "v14", "v15", "v16", "v17", "v18", "v19", "v20", "v21", "v22", "v23",
      "v24", "v25", "v26", "v27", "v28", "v29", "v30", "v31", "v32", "v33", "v34", "v35", "v36", "v37", "v38", "v39", "v40", "v41", "v42", "v43", "v44", "v45", "v46", "v47",
      "v48", "v49", "v50", "v51", "v52", "v53", "v54", "v55", "v56", "v57", "v58", "v59", "v60", "v61", "v62", "v63", "v64", "v65", "v66", "v67", "v68", "v69", "v70", "v71",
      "v72", "v73", "v74", "v75", "v76", "v77", "v78", "v79", "v80", "v81", "v82", "v83", "v84", "v85", "v86", "v87", "v88", "v89", "v90", "v91", "v92", "v93", "v94", "v95",
      "v96", "v97", "v98", "v99", "v100", "v101", "v102", "v103", "v104", "v105", "v106", "v107", "v108", "v109", "v110", "v111", "v112", "v113", "v114", "v115", "v116", "v117", "v118", "v119",
      "v120", "v121", "v122", "v123", "v124", "v125", "v126", "v127", "v128", "v129", "v130", "v131", "v132", "v133", "v134", "v135", "v136", "v137", "v138", "v139", "v140", "v141", "v142", "v143",
      "v144", "v145", "v146", "v147", "v148", "v149", "v150", "v151", "v152", "v153", "v154", "v155", "v156", "v157", "v158", "v159", "v160", "v161", "v162", "v163", "v164", "v165", "v166", "v167",
      "v168", "v169", "v170", "v171", "v172", "v173", "v174", "v175", "v176", "v177", "v178", "v179", "v180", "v181", "v182", "v183", "v184", "v185", "v186", "v187", "v188", "v189", "v190", "v191",
      "v192", "v193", "v194", "v195", "v196", "v197", "v198", "v199", "v200", "v201", "v202", "v203", "v204", "v205", "v206", "v207", "v208", "v209", "v210", "v211", "v212", "v213", "v214", "v215",
      "v216", "v217", "v218", "v219", "v220", "v221", "v222", "v223", "v224", "v225", "v226", "v227", "v228", "v229", "v230", "v231", "v232", "v233", "v234", "v235", "v236", "v237", "v238", "v239",
      "v240", "v241", "v242", "v243", "v244", "v245", "v246", "v247", "v248", "v249", "v250", "v251", "v252", "v253", "v254", "v255", "a0", "a1", "a2", "a3", "a4", "a5", "a6", "a7",
      "a8", "a9", "a10", "a11", "a12", "a13", "a14", "a15", "a16", "a17", "a18", "a19", "a20", "a21", "a22", "a23", "a24", "a25", "a26", "a27", "a28", "a29", "a30", "a31",
      "a32", "a33", "a34", "a35", "a36", "a37", "a38", "a39", "a40", "a41", "a42", "a43", "a44", "a45", "a46", "a47", "a48", "a49", "a50", "a51", "a52", "a53", "a54", "a55",
      "a56", "a57", "a58", "a59", "a60", "a61", "a62", "a63", "a64", "a65", "a66", "a67", "a68", "a69", "a70", "a71", "a72", "a73", "a74", "a75", "a76", "a77", "a78", "a79",
      "a80", "a81", "a82", "a83", "a84", "a85", "a86", "a87", "a88", "a89", "a90", "a91", "a92", "a93", "a94", "a95", "a96", "a97", "a98", "a99", "a100", "a101", "a102", "a103",
      "a104", "a105", "a106", "a107", "a108", "a109", "a110", "a111", "a112", "a113", "a114", "a115", "a116", "a117", "a118", "a119", "a120", "a121", "a122", "a123", "a124", "a125", "a126", "a127",
      "a128", "a129", "a130", "a131", "a132", "a133", "a134", "a135", "a136", "a137", "a138", "a139", "a140", "a141", "a142", "a143", "a144", "a145", "a146", "a147", "a148", "a149", "a150", "a151",
      "a152", "a153", "a154", "a155", "a156", "a157", "a158", "a159", "a160", "a161", "a162", "a163", "a164", "a165", "a166", "a167", "a168", "a169", "a170", "a171", "a172", "a173", "a174", "a175",
      "a176", "a177", "a178", "a179", "a180", "a181", "a182", "a183", "a184", "a185", "a186", "a187", "a188", "a189", "a190", "a191", "a192", "a193", "a194", "a195", "a196", "a197", "a198", "a199",
      "a200", "a201", "a202", "a203", "a204", "a205", "a206", "a207", "a208", "a209", "a210", "a211", "a212", "a213", "a214", "a215", "a216", "a217", "a218", "a219", "a220", "a221", "a222", "a223",
      "a224", "a225", "a226", "a227", "a228", "a229", "a230", "a231", "a232", "a233", "a234", "a235", "a236", "a237", "a238", "a239", "a240", "a241", "a242", "a243", "a244", "a245", "a246", "a247",
      "a248", "a249", "a250", "a251", "a252", "a253", "a254", "a255", "s6", "s7", "s8", "s9", "s10", "s11", "s12", "s13", "s14", "s15", "s16", "s17", "s18", "s19", "s20", "s21",
      "s22", "s23", "s24", "s25", "s26", "s27", "s28", "s29", "s30", "s31", "s33", "s34", "s35", "s36", "s37", "s38", "s39", "s40", "s41", "s42", "s43", "s44", "s45", "s46",
      "s47", "s48", "s49", "s50", "s51", "s52", "s53", "s54", "s55", "s56", "s57", "s58", "s59", "s60", "s61", "s62", "s63", "s64", "s65", "s66", "s67", "s68", "s69", "s70",
      "s71", "s72", "s73", "s74", "s75", "s76", "s77", "s78", "s79", "s80", "s81", "s82", "s83", "s84", "s85", "s86", "s87", "s88", "s89", "s90", "s91", "s92", "s93", "s94",
      "s95", "s96", "s97", "s98", "s99", "vcc", "scc", "memory"
  );
}


extern "C" void kernel_launch(void* const* d_in, const int* in_sizes, int n_in, void* d_out, int out_size, void* d_ws, size_t ws_size,
                              hipStream_t stream) {

  gated_mix_fused<<<256, 256, 0, stream>>>(d_in[0], d_in[1], d_in[2], d_in[4], d_in[5], d_in[6], d_out, d_ws);
}
```
